```python
import torch

def _softmax(x, axis=-1):
    return torch.softmax(x, dim=axis)
def _gelu(x):
    return torch.nn.functional.gelu(x, approximate="tanh")
def _silu(x):
    return torch.nn.functional.silu(x)
def _sigmoid(x):
    return torch.sigmoid(x)
def _layernorm(x, axis=-1, eps=1e-5):
    m = x.mean(dim=axis, keepdim=True)
    v = x.var(dim=axis, keepdim=True, unbiased=False)
    return (x - m) / torch.sqrt(v + eps)
def _scatter_add(tgt, idx, src, axis=0):
    return tgt.clone().index_add_(axis, idx, src)
def _segment_sum(src, seg, n):
    out = torch.zeros((n,) + src.shape[1:], dtype=src.dtype)
    return out.index_add_(0, seg, src)
def _pad(x, pw):
    flat = tuple(v for pair in reversed(pw) for v in pair)
    return torch.nn.functional.pad(x, flat)

kernel_name = 'recurrent_scan__gqa_block_cached__glu_gated_swiglu__decode'

def setup_inputs():

    _w = (lambda a: (lambda t: t.to(torch.bfloat16) if t.is_floating_point() else t)(torch.as_tensor(a)))
    def _A_setup():
        B, Sq, D = 2, 1, 512
        Hq, Hkv, Sk, Dh = 32, 8, 32768, 128
        assert Hq % Hkv == 0, (Hq, Hkv)
        return {
            "x": _w((lambda *s: torch.randn(*s, dtype=torch.bfloat16) * 0.4)(B, Sq, D)),
            "k_cache": _w((lambda *s: torch.randn(*s, dtype=torch.bfloat16) * 1.6)(B, Hkv, Sk, Dh)),
            "v_cache": _w((lambda *s: torch.randn(*s, dtype=torch.bfloat16) * 1.6)(B, Hkv, Sk, Dh)),
            "wq": _w((lambda *s: torch.randn(*s, dtype=torch.bfloat16) * 1.6)(D, Hq * Dh) * (D ** -0.5)),
            "wo": _w((lambda *s: torch.randn(*s, dtype=torch.bfloat16) * 1.6)(Hq * Dh, D) * ((Hq * Dh) ** -0.5)),
        }
    _A_in = _A_setup()

    def _B_setup():
        B, S, D, F = 2, 1, 512, 1792
        return {
            "x": _w((lambda *s: torch.randn(*s, dtype=torch.bfloat16) * 0.4)(B, S, D)),
            "w1": _w((lambda *s: torch.randn(*s, dtype=torch.bfloat16) * 1.6)(D, F) * (2.0 / D) ** 0.5),
            "w3": _w((lambda *s: torch.randn(*s, dtype=torch.bfloat16) * 1.6)(D, F) * (2.0 / D) ** 0.5),
            "w2": _w((lambda *s: torch.randn(*s, dtype=torch.bfloat16) * 1.6)(F, D) * (2.0 / F) ** 0.5),
        }
    _B_in = _B_setup()

    return {
        "A_x": _A_in["x"],
        "A_k_cache": _A_in["k_cache"],
        "A_v_cache": _A_in["v_cache"],
        "A_wq": _A_in["wq"],
        "A_wo": _A_in["wo"],
        "B_x": _B_in["x"],
        "B_w1": _B_in["w1"],
        "B_w3": _B_in["w3"],
        "B_w2": _B_in["w2"],
    }

def reference(A_x, A_k_cache, A_v_cache, A_wq, A_wo, B_x, B_w1, B_w3, B_w2):
    def _A_ref(x, k_cache, v_cache, wq, wo):
        B, Sq, D = x.shape
        Hq, Dh = 32, 128
        Hkv = k_cache.shape[1]
        rep = Hq // Hkv
        q = (lambda x, *d: x.permute(*d))((x @ wq).reshape(B, Sq, Hq, Dh), 0, 2, 1, 3)
        k = (lambda x, n, axis: torch.repeat_interleave(x, n, dim=axis))(k_cache, rep, axis=1)
        v = (lambda x, n, axis: torch.repeat_interleave(x, n, dim=axis))(v_cache, rep, axis=1)
        scores = torch.einsum("bhqd,bhkd->bhqk", q, k) * (Dh ** -0.5)
        p = _softmax(scores, axis=-1)
        o = torch.einsum("bhqk,bhkd->bhqd", p, v)
        return (lambda x, *d: x.permute(*d))(o, 0, 2, 1, 3).reshape(B, Sq, Hq * Dh) @ wo

    def _B_ref(x, w1, w3, w2):
        gate = torch.einsum("bsd,df->bsf", x, w1)
        up = torch.einsum("bsd,df->bsf", x, w3)
        h = _silu(gate) * up
        out = torch.einsum("bsf,fd->bsd", h, w2)
        return out

    x = A_x
    xn = _layernorm(x, axis=-1)
    s = _A_ref(xn, A_k_cache, A_v_cache, A_wq, A_wo)
    g = _silu(_B_ref(xn, B_w1, B_w3, B_w2))
    return x + s * g
```

```cpp
#include <hip/hip_runtime.h>
#include <hip/hip_bf16.h>
#include <stdint.h>

#define GAS __attribute__((address_space(1)))
#define LAS __attribute__((address_space(3)))
typedef unsigned u32x4 __attribute__((ext_vector_type(4)));
typedef unsigned u32x2 __attribute__((ext_vector_type(2)));
typedef float f32x4 __attribute__((ext_vector_type(4)));
typedef short s16x8 __attribute__((ext_vector_type(8)));
typedef short s16x4 __attribute__((ext_vector_type(4)));
typedef __bf16 bf16x2_t __attribute__((ext_vector_type(2)));

constexpr int DM = 512, HQ = 32, HKV = 8, SK = 32768, DH = 128, FF = 1792;
constexpr int NWG = 256, NTHR = 512, NWAVE = 8, NSPLIT = 16;
constexpr int KEYS_WG = SK / NSPLIT;
constexpr int TILE = 32, NTILE = KEYS_WG / (TILE * NWAVE);
constexpr int NFFP = FF / 8;
constexpr float SCALE = 0.08838834764831845f;
constexpr float LOG2E = 1.4426950408889634f;

constexpr size_t WS_Q = 0;
constexpr size_t WS_FFN = 16384;
constexpr size_t WS_PO = WS_FFN + (size_t)NFFP * 1024 * 4;
constexpr size_t WS_PML = WS_PO + (size_t)NWG * 512 * 4;
constexpr size_t WS_SP = WS_PML + (size_t)NWG * 8 * 4;

__device__ __forceinline__ float asf(unsigned u) { return __builtin_bit_cast(float, u); }
__device__ __forceinline__ unsigned asu(float f) { return __builtin_bit_cast(unsigned, f); }
__device__ __forceinline__ unsigned pack2(float lo, float hi) {
  bf16x2_t v = {(__bf16)lo, (__bf16)hi};
  return __builtin_bit_cast(unsigned, v);
}
__device__ __forceinline__ float rbf(float x) { return (float)(__bf16)x; }
__device__ __forceinline__ float blo(unsigned u) { return asf(u << 16); }
__device__ __forceinline__ float bhi(unsigned u) { return asf(u & 0xffff0000u); }
__device__ __forceinline__ f32x4 mfma16(u32x4 a, u32x4 b, f32x4 c) {
  return __builtin_amdgcn_mfma_f32_16x16x32_bf16(__builtin_bit_cast(s16x8, a), __builtin_bit_cast(s16x8, b), c, 0, 0, 0);
}
template <int CTRL> __device__ __forceinline__ float dppf(float x) {
  return asf(__builtin_amdgcn_mov_dpp(asu(x), CTRL, 0xf, 0xf, true));
}
template <int CTRL> __device__ __forceinline__ unsigned dppu(unsigned x) {
  return __builtin_amdgcn_mov_dpp(x, CTRL, 0xf, 0xf, true);
}
template <int CTRL, int BANK> __device__ __forceinline__ float updf(float old, float x) {
  return asf(__builtin_amdgcn_update_dpp(asu(old), asu(x), CTRL, 0xf, BANK, false));
}
constexpr int ROR4 = 0x124, ROR8 = 0x128, ROR12 = 0x12C, QID = 0xE4, XOR1 = 0xB1, XOR2 = 0x4E;
__device__ __forceinline__ float xrow_max(float x) {
  auto s = __builtin_amdgcn_permlane16_swap(asu(x), asu(x), false, false);
  x = fmaxf(asf(s[0]), asf(s[1]));
  auto t = __builtin_amdgcn_permlane32_swap(asu(x), asu(x), false, false);
  return fmaxf(asf(t[0]), asf(t[1]));
}
__device__ __forceinline__ float xrow_sum(float x) {
  auto s = __builtin_amdgcn_permlane16_swap(asu(x), asu(x), false, false);
  x = asf(s[0]) + asf(s[1]);
  auto t = __builtin_amdgcn_permlane32_swap(asu(x), asu(x), false, false);
  return asf(t[0]) + asf(t[1]);
}
__device__ __forceinline__ float wave_sum(float x) {
  x += dppf<XOR1>(x); x += dppf<XOR2>(x); x += dppf<ROR4>(x); x += dppf<ROR8>(x);
  return xrow_sum(x);
}
__device__ __forceinline__ u32x2 tr_read(LAS const unsigned char* p) {
  return __builtin_bit_cast(u32x2, __builtin_amdgcn_ds_read_tr16_b64_v4i16((LAS s16x4*)p));
}
__device__ __forceinline__ float silu(float x) { return x / (1.0f + __expf(-x)); }

__device__ __forceinline__ u32x4 layernorm_row(u32x4 xr) {
  float v[8];
#pragma unroll
  for (int j = 0; j < 4; ++j) { v[2 * j] = blo(xr[j]); v[2 * j + 1] = bhi(xr[j]); }
  float s = 0.f;
#pragma unroll
  for (int j = 0; j < 8; ++j) s += v[j];
  const float mean = wave_sum(s) * (1.0f / DM);
  float q = 0.f;
#pragma unroll
  for (int j = 0; j < 8; ++j) { const float d = v[j] - mean; q += d * d; }
  const float var = wave_sum(q) * (1.0f / DM);
  const float mb = rbf(mean);
  const float den = rbf(sqrtf(rbf(rbf(var) + 1e-5f)));
  u32x4 o;
#pragma unroll
  for (int j = 0; j < 4; ++j) o[j] = pack2(rbf(v[2 * j] - mb) / den, rbf(v[2 * j + 1] - mb) / den);
  return o;
}

__global__ void __launch_bounds__(NTHR, 2) k_prep(const __hip_bfloat16* __restrict__ x_, const __hip_bfloat16* __restrict__ wq_,
                                                  const __hip_bfloat16* __restrict__ w1_, const __hip_bfloat16* __restrict__ w3_,
                                                  const __hip_bfloat16* __restrict__ w2_, unsigned char* __restrict__ ws) {
  __shared__ __attribute__((aligned(16))) unsigned char lds[2048 + 8 * 2048 + 8 * 2048 + 8 * 1056 + 2048 + 64];
  LAS unsigned char* L = (LAS unsigned char*)lds;
  constexpr int O_XN = 0, O_WQ = 2048, O_W13 = O_WQ + 16384, O_W2 = O_W13 + 16384, O_PART = O_W2 + 8 * 1056, O_H = O_PART + 2048;
  const int bid = blockIdx.x, kvh = bid & 7, j = bid >> 3;
  const int tid = threadIdx.x, l = tid & 63, w = __builtin_amdgcn_readfirstlane(tid >> 6);
  const bool has_ffn = j < 28;
  const int fs = kvh * 28 + j;
  const GAS unsigned char* x = (const GAS unsigned char*)x_;
  const u32x4 x0 = *(const GAS u32x4*)(x + l * 16), x1 = *(const GAS u32x4*)(x + 1024 + l * 16);
  u32x4 wqr[2], w13r[2], w2r;
#pragma unroll
  for (int i = 0; i < 2; ++i) {
    const int row = 64 * w + 32 * i + (l >> 1);
    wqr[i] = *(const GAS u32x4*)((const GAS unsigned char*)wq_ + ((size_t)row * 4096 + kvh * 512 + j * 16 + (l & 1) * 8) * 2);
  }
  if (has_ffn) {
#pragma unroll
    for (int i = 0; i < 2; ++i) {
      const int row = 64 * w + 32 * i + (l >> 1);
      const GAS unsigned char* base = (const GAS unsigned char*)((l & 1) ? w3_ : w1_);
      w13r[i] = *(const GAS u32x4*)(base + ((size_t)row * FF + fs * 8) * 2);
    }
    w2r = *(const GAS u32x4*)((const GAS unsigned char*)w2_ + ((size_t)(fs * 8 + w) * 512 + l * 8) * 2);
  } else {
    w13r[0] = w13r[1] = w2r = u32x4{0, 0, 0, 0};
  }
  const u32x4 xn0 = layernorm_row(x0), xn1 = layernorm_row(x1);
  if ((l >> 3) == w) {
    *(LAS u32x4*)(L + O_XN + l * 16) = xn0;
    *(LAS u32x4*)(L + O_XN + 1024 + l * 16) = xn1;
  }
#pragma unroll
  for (int i = 0; i < 2; ++i) {
    *(LAS u32x4*)(L + O_WQ + w * 2048 + i * 1024 + l * 16) = wqr[i];
    *(LAS u32x4*)(L + O_W13 + w * 2048 + i * 1024 + l * 16) = w13r[i];
  }
  *(LAS u32x4*)(L + O_W2 + w * 1056 + l * 16) = w2r;
  __builtin_amdgcn_fence(__ATOMIC_RELEASE, "wavefront");
  __builtin_amdgcn_wave_barrier();
  const int g = l >> 4, qq = (l & 15) >> 2, p = l & 3;
  f32x4 dq = {0, 0, 0, 0}, df = {0, 0, 0, 0};
#pragma unroll
  for (int s = 0; s < 2; ++s) {
    const u32x4 af = *(LAS const u32x4*)(L + O_XN + (l & 1) * 1024 + (64 * w + 32 * s + 8 * g) * 2);
    const int ro = (32 * s + 8 * g + qq) * 32 + p * 8;
    const u32x2 b0 = tr_read(L + O_WQ + w * 2048 + ro), b1 = tr_read(L + O_WQ + w * 2048 + ro + 128);
    dq = mfma16(af, u32x4{b0[0], b0[1], b1[0], b1[1]}, dq);
    const u32x2 c0 = tr_read(L + O_W13 + w * 2048 + ro), c1 = tr_read(L + O_W13 + w * 2048 + ro + 128);
    df = mfma16(af, u32x4{c0[0], c0[1], c1[0], c1[1]}, df);
  }
  LAS float* part = (LAS float*)(L + O_PART);
  if (l < 16) {
    part[w * 64 + l] = dq[0]; part[w * 64 + 16 + l] = dq[1];
    part[w * 64 + 32 + l] = df[0]; part[w * 64 + 48 + l] = df[1];
  }
  __syncthreads();
  if (tid < 64) {
    float s = 0.f;
#pragma unroll
    for (int ww = 0; ww < 8; ++ww) s += part[ww * 64 + tid];
    s = rbf(s);
    if (tid < 32) {
      const int b = tid >> 4, c = tid & 15;
      ((GAS __bf16*)(ws + WS_Q))[b * 4096 + kvh * 512 + j * 16 + c] = (__bf16)s;
    }
    const float up = dppf<ROR8>(s);
    if (tid >= 32 && (tid & 8) == 0) {
      const float h = rbf(rbf(silu(s)) * up);
      ((LAS __bf16*)(L + O_H))[((tid >> 4) & 1) * 8 + (tid & 7)] = (__bf16)h;
    }
  }
  __syncthreads();
  if (has_ffn) {
    u32x4 af = *(LAS const u32x4*)(L + O_H + (l & 1) * 16);
    if (g != 0) af = u32x4{0, 0, 0, 0};
    f32x4 o0, o1;
#pragma unroll
    for (int n = 0; n < 4; ++n) {
      const int ro = qq * 1056 + (64 * w + 16 * n + 4 * p) * 2;
      const u32x2 b0 = tr_read(L + O_W2 + ro), b1 = tr_read(L + O_W2 + ro + 4 * 1056);
      const f32x4 d = mfma16(af, u32x4{b0[0], b0[1], b1[0], b1[1]}, f32x4{0, 0, 0, 0});
      o0[n] = d[0]; o1[n] = d[1];
    }
    if (l < 16) {
      GAS f32x4* fp = (GAS f32x4*)(ws + WS_FFN) + (size_t)fs * 256;
      fp[w * 16 + l] = o0;
      fp[128 + w * 16 + l] = o1;
    }
  }
}

__device__ __forceinline__ u32x4 ldnt(const GAS u32x4* p) { return __builtin_nontemporal_load(p); }

__device__ __forceinline__ float qk_reduce(const f32x4 d0, const f32x4 d1, const f32x4 d2, const f32x4 d3) {
  float z0 = d0[0];
  z0 = updf<QID, 0x2>(z0, d1[1]); z0 = updf<QID, 0x4>(z0, d2[2]); z0 = updf<QID, 0x8>(z0, d3[3]);
  float z1 = dppf<ROR4>(d0[3]);
  z1 = updf<ROR4, 0x2>(z1, d1[0]); z1 = updf<ROR4, 0x4>(z1, d2[1]); z1 = updf<ROR4, 0x8>(z1, d3[2]);
  float z2 = dppf<ROR8>(d0[2]);
  z2 = updf<ROR8, 0x2>(z2, d1[3]); z2 = updf<ROR8, 0x4>(z2, d2[0]); z2 = updf<ROR8, 0x8>(z2, d3[1]);
  float z3 = dppf<ROR12>(d0[1]);
  z3 = updf<ROR12, 0x2>(z3, d1[2]); z3 = updf<ROR12, 0x4>(z3, d2[3]); z3 = updf<ROR12, 0x8>(z3, d3[0]);
  return (z0 + z1) + (z2 + z3);
}

__global__ void __launch_bounds__(NTHR, 2) k_attn(const __hip_bfloat16* __restrict__ k_, const __hip_bfloat16* __restrict__ v_,
                                                  unsigned char* __restrict__ ws) {
  __shared__ __attribute__((aligned(16))) float lds[8 * 512 + 64];
  const int bid = blockIdx.x, kvh = bid & 7, j = bid >> 3, b = j >> 4, sp = j & 15;
  const int tid = threadIdx.x, l = tid & 63, w = __builtin_amdgcn_readfirstlane(tid >> 6);
  const int g = l >> 4, a = (l >> 2) & 3, u = l & 3;
  const size_t base = ((size_t)(b * HKV + kvh) * SK + sp * KEYS_WG) * 16;
  const GAS u32x4* kp = (const GAS u32x4*)k_ + base + w * (TILE * 16) + (a * 16 + g * 4 + u);
  const GAS u32x4* vp = (const GAS u32x4*)v_ + base + w * (TILE * 16) + l;
  constexpr int TSTR = NWAVE * TILE * 16;
  const int ucol = (l >> 2) & 3, h = l & 3;
  const u32x4 qf = *(const GAS u32x4*)(ws + WS_Q + ((size_t)b * 4096 + (kvh * 4 + h) * 128 + (4 * g + ucol) * 8) * 2);
  __builtin_amdgcn_sched_barrier(0);
  u32x4 K[2][8], V[2][8];
#pragma unroll
  for (int i = 0; i < 2; ++i) {
#pragma unroll
    for (int t = 0; t < 8; ++t) K[i][t] = ldnt(kp + i * TSTR + t * 64);
#pragma unroll
    for (int t = 0; t < 8; ++t) V[i][t] = ldnt(vp + i * TSTR + t * 64);
  }
  __builtin_amdgcn_sched_barrier(0);
  f32x4 acc[8];
#pragma unroll
  for (int e = 0; e < 8; ++e) acc[e] = f32x4{0, 0, 0, 0};
  float m = -INFINITY, lsum = 0.f;
#pragma unroll
  for (int i = 0; i < NTILE; ++i) {
    const int c = i & 1;
    f32x4 d[8];
#pragma unroll
    for (int t = 0; t < 8; ++t) d[t] = mfma16(K[c][t], qf, f32x4{0, 0, 0, 0});
    if (i + 2 < NTILE) {
#pragma unroll
      for (int t = 0; t < 8; ++t) K[c][t] = ldnt(kp + (i + 2) * TSTR + t * 64);
    }
    const float S0 = qk_reduce(d[0], d[1], d[2], d[3]), S1 = qk_reduce(d[4], d[5], d[6], d[7]);
    unsigned pk = pack2(S0, S1);
    pk = pack2(blo(pk) * SCALE, bhi(pk) * SCALE);
    const float a0 = blo(pk), a1 = bhi(pk);
    float tm = fmaxf(a0, a1);
    tm = fmaxf(tm, dppf<ROR4>(tm)); tm = fmaxf(tm, dppf<ROR8>(tm)); tm = xrow_max(tm);
    if (i == 0) {
      m = tm;
    } else if (__any(tm > m)) {
      const float mn = fmaxf(m, tm);
      const float al = __builtin_amdgcn_exp2f((m - mn) * LOG2E);
      m = mn; lsum *= al;
      const float al0 = dppf<0x00>(al), al1 = dppf<0x55>(al), al2 = dppf<0xAA>(al), al3 = dppf<0xFF>(al);
#pragma unroll
      for (int e = 0; e < 8; ++e) { acc[e][0] *= al0; acc[e][1] *= al1; acc[e][2] *= al2; acc[e][3] *= al3; }
    }
    const float p0 = __builtin_amdgcn_exp2f((a0 - m) * LOG2E), p1 = __builtin_amdgcn_exp2f((a1 - m) * LOG2E);
    lsum += p0 + p1;
    const unsigned pw = pack2(p0, p1);
    const u32x4 pf = {pw, dppu<ROR12>(pw), dppu<ROR8>(pw), dppu<ROR4>(pw)};
#pragma unroll
    for (int e = 0; e < 8; ++e) {
      const int r = e >> 1;
      const unsigned sel = (e & 1) ? 0x07060302u : 0x05040100u;
      const u32x4 bf = {__builtin_amdgcn_perm(V[c][4][r], V[c][0][r], sel), __builtin_amdgcn_perm(V[c][5][r], V[c][1][r], sel),
                        __builtin_amdgcn_perm(V[c][6][r], V[c][2][r], sel), __builtin_amdgcn_perm(V[c][7][r], V[c][3][r], sel)};
      acc[e] = mfma16(pf, bf, acc[e]);
    }
    if (i + 2 < NTILE) {
#pragma unroll
      for (int t = 0; t < 8; ++t) V[c][t] = ldnt(vp + (i + 2) * TSTR + t * 64);
    }
    __builtin_amdgcn_sched_barrier(0);
  }
  lsum += dppf<ROR4>(lsum); lsum += dppf<ROR8>(lsum); lsum = xrow_sum(lsum);
  float* rec = lds + w * 512;
  float* ml = lds + 8 * 512;
  if (l < 16) {
#pragma unroll
    for (int hh = 0; hh < 4; ++hh) {
      *(f32x4*)(rec + hh * 128 + 8 * l) = f32x4{acc[0][hh], acc[1][hh], acc[2][hh], acc[3][hh]};
      *(f32x4*)(rec + hh * 128 + 8 * l + 4) = f32x4{acc[4][hh], acc[5][hh], acc[6][hh], acc[7][hh]};
    }
    if (l < 4) { ml[w * 8 + l * 2] = m; ml[w * 8 + l * 2 + 1] = lsum; }
  }
  __syncthreads();
  {
    const int hh = tid >> 7;
    float M = -INFINITY;
#pragma unroll
    for (int ww = 0; ww < 8; ++ww) M = fmaxf(M, ml[ww * 8 + hh * 2]);
    float o = 0.f, Ls = 0.f;
#pragma unroll
    for (int ww = 0; ww < 8; ++ww) {
      const float f = __builtin_amdgcn_exp2f((ml[ww * 8 + hh * 2] - M) * LOG2E);
      o += f * lds[ww * 512 + tid];
      Ls += f * ml[ww * 8 + hh * 2 + 1];
    }
    ((GAS float*)(ws + WS_PO))[bid * 512 + tid] = o;
    if ((tid & 127) == 0) { ((GAS float*)(ws + WS_PML))[bid * 8 + hh * 2] = M; ((GAS float*)(ws + WS_PML))[bid * 8 + hh * 2 + 1] = Ls; }
  }
}

__global__ void __launch_bounds__(NTHR, 2) k_tail1(const __hip_bfloat16* __restrict__ wo_, unsigned char* __restrict__ ws) {
  __shared__ __attribute__((aligned(16))) unsigned char lds[32 * 1056 + 8 * 32 * 4 + 64];
  LAS unsigned char* L = (LAS unsigned char*)lds;
  constexpr int O_WO = 0, O_RED = 32 * 1056, O_O = O_RED + 1024;
  const int bid = blockIdx.x, kvh = bid & 7, j = bid >> 3, b = j >> 4, sp = j & 15;
  const int tid = threadIdx.x, l = tid & 63, w = __builtin_amdgcn_readfirstlane(tid >> 6);
  u32x4 wr[4];
#pragma unroll
  for (int i = 0; i < 4; ++i)
    wr[i] = *(const GAS u32x4*)((const GAS unsigned char*)wo_ + ((size_t)(kvh * 512 + 32 * sp + 4 * w + i) * 512 + l * 8) * 2);
  const int p = tid >> 5, i5 = tid & 31, hh = sp >> 2;
  const int pbid = ((b * 16 + p) << 3) | kvh;
  const float ov = ((const GAS float*)(ws + WS_PO))[pbid * 512 + 32 * sp + i5];
  const int lb = ((b * 16 + (l & 15)) << 3) | kvh;
  const float mp = ((const GAS float*)(ws + WS_PML))[lb * 8 + hh * 2], lp = ((const GAS float*)(ws + WS_PML))[lb * 8 + hh * 2 + 1];
#pragma unroll
  for (int i = 0; i < 4; ++i) *(LAS u32x4*)(L + O_WO + (4 * w + i) * 1056 + l * 16) = wr[i];
  float M = mp;
  M = fmaxf(M, dppf<XOR1>(M)); M = fmaxf(M, dppf<XOR2>(M)); M = fmaxf(M, dppf<ROR4>(M)); M = fmaxf(M, dppf<ROR8>(M));
  const float f = __builtin_amdgcn_exp2f((mp - M) * LOG2E);
  float Ls = f * lp;
  Ls += dppf<XOR1>(Ls); Ls += dppf<XOR2>(Ls); Ls += dppf<ROR4>(Ls); Ls += dppf<ROR8>(Ls);
  const float fp = __shfl(f, p);
  float o = ov * fp;
  { auto t = __builtin_amdgcn_permlane32_swap(asu(o), asu(o), false, false); o = asf(t[0]) + asf(t[1]); }
  if (l < 32) ((LAS float*)(L + O_RED))[w * 32 + l] = o;
  __syncthreads();
  if (tid < 32) {
    float s = 0.f;
#pragma unroll
    for (int ww = 0; ww < 8; ++ww) s += ((LAS float*)(L + O_RED))[ww * 32 + tid];
    ((LAS __bf16*)(L + O_O))[tid] = (__bf16)(s / Ls);
  }
  __syncthreads();
  const int g = l >> 4, qq = (l & 15) >> 2, pp = l & 3;
  const u32x4 af = *(LAS const u32x4*)(L + O_O + g * 16);
  f32x4 so;
#pragma unroll
  for (int n = 0; n < 4; ++n) {
    const int ro = (8 * g + qq) * 1056 + (64 * w + 16 * n + 4 * pp) * 2;
    const u32x2 b0 = tr_read(L + O_WO + ro), b1 = tr_read(L + O_WO + ro + 4 * 1056);
    const f32x4 d = mfma16(af, u32x4{b0[0], b0[1], b1[0], b1[1]}, f32x4{0, 0, 0, 0});
    so[n] = d[0];
  }
  if (l < 16) {
    const int cw = w * 16 + l, pw = kvh * 16 + sp;
    ((GAS f32x4*)(ws + WS_SP))[((size_t)(b * 128 + cw)) * 128 + pw] = so;
  }
}

__global__ void __launch_bounds__(NTHR, 2) k_tail2(const __hip_bfloat16* __restrict__ x_, unsigned char* __restrict__ ws,
                                                   __hip_bfloat16* __restrict__ out) {
  __shared__ float red[2][8][4];
  const int bid = blockIdx.x, b = bid >> 7, cw = bid & 127;
  const int tid = threadIdx.x, l = tid & 63, w = tid >> 6;
  float sv = ((const GAS float*)(ws + WS_SP))[((size_t)(b * 128 + cw)) * 512 + tid];
  float fv = 0.f;
  {
    const int p0 = tid >> 2, n = tid & 3;
    fv = ((const GAS float*)(ws + WS_FFN))[(size_t)p0 * 1024 + b * 512 + cw * 4 + n];
    if (p0 + 128 < NFFP) fv += ((const GAS float*)(ws + WS_FFN))[(size_t)(p0 + 128) * 1024 + b * 512 + cw * 4 + n];
  }
  sv += dppf<ROR4>(sv); sv += dppf<ROR8>(sv); sv = xrow_sum(sv);
  fv += dppf<ROR4>(fv); fv += dppf<ROR8>(fv); fv = xrow_sum(fv);
  if (l < 4) { red[0][w][l] = sv; red[1][w][l] = fv; }
  __syncthreads();
  if (tid < 4) {
    float s = 0.f, f = 0.f;
#pragma unroll
    for (int ww = 0; ww < 8; ++ww) { s += red[0][ww][tid]; f += red[1][ww][tid]; }
    const int col = 64 * (cw >> 4) + 16 * tid + (cw & 15);
    const float gg = rbf(silu(rbf(f)));
    const float xv = (float)((const GAS __bf16*)x_)[b * 512 + col];
    ((GAS __bf16*)out)[b * 512 + col] = (__bf16)(xv + rbf(rbf(s) * gg));
  }
}

extern "C" void kernel_launch(void* const* d_in, const int* in_sizes, int n_in, void* d_out, int out_size, void* d_ws, size_t ws_size,
                              hipStream_t stream) {
  const __hip_bfloat16* x = (const __hip_bfloat16*)d_in[0];
  const __hip_bfloat16* kc = (const __hip_bfloat16*)d_in[1];
  const __hip_bfloat16* vc = (const __hip_bfloat16*)d_in[2];
  const __hip_bfloat16* wq = (const __hip_bfloat16*)d_in[3];
  const __hip_bfloat16* wo = (const __hip_bfloat16*)d_in[4];
  const __hip_bfloat16* w1 = (const __hip_bfloat16*)d_in[6];
  const __hip_bfloat16* w3 = (const __hip_bfloat16*)d_in[7];
  const __hip_bfloat16* w2 = (const __hip_bfloat16*)d_in[8];
  unsigned char* ws = (unsigned char*)d_ws;
  k_prep<<<NWG, NTHR, 0, stream>>>(x, wq, w1, w3, w2, ws);
  k_attn<<<NWG, NTHR, 0, stream>>>(kc, vc, ws);
  k_tail1<<<NWG, NTHR, 0, stream>>>(wo, ws);
  k_tail2<<<NWG, NTHR, 0, stream>>>(x, ws, (__hip_bfloat16*)d_out);
}
```

```cpp
#include <hip/hip_runtime.h>
#include <hip/hip_bf16.h>
#include <stdint.h>

#define DIAG 0
#define QPOLL_T 550
#define TOUCH 0
#define TSLEEP 2
#define NO_FFN 0
#define LEAD 4
#define H2_OUT 1
#define X_NOPAY 0
#define X_NOGU 0
#define X_NOW13 0
#define NO_WO 0

#define GAS __attribute__((address_space(1)))
#define LAS __attribute__((address_space(3)))
typedef unsigned u32x4 __attribute__((ext_vector_type(4)));
typedef unsigned u32x2 __attribute__((ext_vector_type(2)));
typedef float f32x4 __attribute__((ext_vector_type(4)));
typedef short s16x8 __attribute__((ext_vector_type(8)));
typedef short s16x4 __attribute__((ext_vector_type(4)));
typedef __bf16 bf16x2_t __attribute__((ext_vector_type(2)));
typedef unsigned long long u64;
typedef GAS u64 gu64;

constexpr int DM = 512, HKV = 8, SK = 32768, FF = 1792;
constexpr int NWG = 256, NTHR = 512, NSPLIT = 16;
constexpr int KEYS_WG = SK / NSPLIT;
constexpr int NTILE = KEYS_WG / (32 * 4);
constexpr int NFFP = FF / 8;
constexpr float SCALE = 0.08838834764831845f;
constexpr float LOG2E = 1.4426950408889634f;
constexpr unsigned SPIN_CAP = 1u << 15;
constexpr unsigned INVAL = 0xFFFFFFFFu;

constexpr int WS_EPOCH = 0;
constexpr int WS_FLAG = 2048;
constexpr int WS_QBOX = 8192;
constexpr int WS_HB = WS_QBOX + 65536;
constexpr int WS_IN = WS_HB + 8192;
constexpr int IN_H1 = 0;
constexpr int IN_H2 = 2048;
constexpr int IN_GU = 4096;
constexpr int IN_F2 = 6144;
constexpr int IN_ML = 6656;
constexpr int IN_STR = 7168;
constexpr int WS_END = WS_IN + NWG * IN_STR;
constexpr size_t WS_DIAG = WS_END;

constexpr int WO_STR = 1056;
constexpr int O_XN = 0;
constexpr int O_WQ = 2048;
constexpr int W2_STR = 160;
constexpr int O_W2 = O_WQ + 16384;
constexpr int O_GU = O_W2 + 56 * W2_STR + 64;
constexpr int O_PART = O_GU + 16 * 144;
constexpr int O_H = O_PART + 512;
constexpr int O_Q = O_H + 256;
constexpr int O_WO = O_Q + 1024;
constexpr int O_REC = O_WO + 32 * WO_STR;
constexpr int O_MLW = O_REC + 8192;
constexpr int O_O = O_MLW + 128;
constexpr int O_FG = O_O + 512;
constexpr int O_CNT = O_FG + 16;
constexpr int LDS_BYTES = 84 * 1024;
static_assert(O_CNT + 32 <= LDS_BYTES, "");

#define RLX_AGENT __ATOMIC_RELAXED, __HIP_MEMORY_SCOPE_AGENT
__device__ __forceinline__ float asf(unsigned u) { return __builtin_bit_cast(float, u); }
__device__ __forceinline__ unsigned asu(float f) { return __builtin_bit_cast(unsigned, f); }
__device__ __forceinline__ unsigned san(float f) { const unsigned u = asu(f); return u == INVAL ? 0x7FC00000u : u; }
__device__ __forceinline__ u32x4 san4(f32x4 v) { return u32x4{san(v[0]), san(v[1]), san(v[2]), san(v[3])}; }
__device__ __forceinline__ bool ok4(u32x4 v) { return v[0] != INVAL && v[1] != INVAL && v[2] != INVAL && v[3] != INVAL; }
__device__ __forceinline__ unsigned pack2(float lo, float hi) {
  bf16x2_t v = {(__bf16)lo, (__bf16)hi};
  return __builtin_bit_cast(unsigned, v);
}
__device__ __forceinline__ float rbf(float x) { return (float)(__bf16)x; }
__device__ __forceinline__ float blo(unsigned u) { return asf(u << 16); }
__device__ __forceinline__ float bhi(unsigned u) { return asf(u & 0xffff0000u); }
__device__ __forceinline__ f32x4 mfma16(u32x4 a, u32x4 b, f32x4 c) {
  return __builtin_amdgcn_mfma_f32_16x16x32_bf16(__builtin_bit_cast(s16x8, a), __builtin_bit_cast(s16x8, b), c, 0, 0, 0);
}
template <int CTRL> __device__ __forceinline__ float dppf(float x) {
  return asf(__builtin_amdgcn_mov_dpp(asu(x), CTRL, 0xf, 0xf, true));
}
template <int CTRL> __device__ __forceinline__ unsigned dppu(unsigned x) {
  return __builtin_amdgcn_mov_dpp(x, CTRL, 0xf, 0xf, true);
}
template <int CTRL, int BANK> __device__ __forceinline__ float updf(float old, float x) {
  return asf(__builtin_amdgcn_update_dpp(asu(old), asu(x), CTRL, 0xf, BANK, false));
}
constexpr int ROR4 = 0x124, ROR8 = 0x128, ROR12 = 0x12C, QID = 0xE4, XOR1 = 0xB1, XOR2 = 0x4E;
__device__ __forceinline__ float xrow_max(float x) {
  auto s = __builtin_amdgcn_permlane16_swap(asu(x), asu(x), false, false);
  x = fmaxf(asf(s[0]), asf(s[1]));
  auto t = __builtin_amdgcn_permlane32_swap(asu(x), asu(x), false, false);
  return fmaxf(asf(t[0]), asf(t[1]));
}
__device__ __forceinline__ float xrow_sum(float x) {
  auto s = __builtin_amdgcn_permlane16_swap(asu(x), asu(x), false, false);
  x = asf(s[0]) + asf(s[1]);
  auto t = __builtin_amdgcn_permlane32_swap(asu(x), asu(x), false, false);
  return asf(t[0]) + asf(t[1]);
}
__device__ __forceinline__ float wave_sum(float x) {
  x += dppf<XOR1>(x); x += dppf<XOR2>(x); x += dppf<ROR4>(x); x += dppf<ROR8>(x);
  return xrow_sum(x);
}
__device__ __forceinline__ u32x2 tr_read(LAS const unsigned char* p) {
  return __builtin_bit_cast(u32x2, __builtin_amdgcn_ds_read_tr16_b64_v4i16((LAS s16x4*)p));
}
__device__ __forceinline__ float silu(float x) { return x / (1.0f + __expf(-x)); }
__device__ __forceinline__ void wg_barrier() { asm volatile("s_waitcnt lgkmcnt(0)\n\ts_barrier" ::: "memory"); }
__device__ __forceinline__ void lds_wait() { asm volatile("s_waitcnt lgkmcnt(0)" ::: "memory"); }
__device__ __forceinline__ void cfence() { asm volatile("" ::: "memory"); __builtin_amdgcn_sched_barrier(0); }
__device__ __forceinline__ u32x4 ldnt(const GAS unsigned char* p) { return __builtin_nontemporal_load((const GAS u32x4*)p); }
__device__ __forceinline__ u64 ld_sc1(const GAS u64* p) { return __hip_atomic_load((gu64*)p, RLX_AGENT); }
__device__ __forceinline__ void st_sc1(GAS u64* p, u64 v) { __hip_atomic_store(p, v, RLX_AGENT); }
constexpr int AUX_SC1 = 16, AUX_NT = 2;
#define LD16(off) __builtin_amdgcn_raw_buffer_load_b128(rs, (off), 0, AUX_SC1)
#define LD8(off) __builtin_amdgcn_raw_buffer_load_b64(rs, (off), 0, AUX_SC1)
#define ST16(v, off) __builtin_amdgcn_raw_buffer_store_b128((v), rs, (off), 0, AUX_SC1)
#define ST8(v, off) __builtin_amdgcn_raw_buffer_store_b64((v), rs, (off), 0, AUX_SC1)
__device__ __forceinline__ void lds_arrive(LAS unsigned* c, int l) {
  asm volatile("s_waitcnt lgkmcnt(0)" ::: "memory");
  if (l == 0) __hip_atomic_fetch_add(c, 1u, __ATOMIC_RELAXED, __HIP_MEMORY_SCOPE_WORKGROUP);
}
__device__ __forceinline__ void lds_wait_cnt(LAS unsigned* c, unsigned n) {
  while (__builtin_amdgcn_readfirstlane(__hip_atomic_load(c, __ATOMIC_RELAXED, __HIP_MEMORY_SCOPE_WORKGROUP)) < n) __builtin_amdgcn_s_sleep(1);
  asm volatile("" ::: "memory");
}

__device__ __forceinline__ float qk_reduce(const f32x4 d0, const f32x4 d1, const f32x4 d2, const f32x4 d3) {
  float z0 = d0[0];
  z0 = updf<QID, 0x2>(z0, d1[1]); z0 = updf<QID, 0x4>(z0, d2[2]); z0 = updf<QID, 0x8>(z0, d3[3]);
  float z1 = dppf<ROR4>(d0[3]);
  z1 = updf<ROR4, 0x2>(z1, d1[0]); z1 = updf<ROR4, 0x4>(z1, d2[1]); z1 = updf<ROR4, 0x8>(z1, d3[2]);
  float z2 = dppf<ROR8>(d0[2]);
  z2 = updf<ROR8, 0x2>(z2, d1[3]); z2 = updf<ROR8, 0x4>(z2, d2[0]); z2 = updf<ROR8, 0x8>(z2, d3[1]);
  float z3 = dppf<ROR12>(d0[1]);
  z3 = updf<ROR12, 0x2>(z3, d1[2]); z3 = updf<ROR12, 0x4>(z3, d2[3]); z3 = updf<ROR12, 0x8>(z3, d3[0]);
  return (z0 + z1) + (z2 + z3);
}

#define DIAG_RETRY
#define STAMP(i) do { } while (0)

__global__ void __launch_bounds__(NTHR, 2)
k_fused(const __hip_bfloat16* __restrict__ x_, const __hip_bfloat16* __restrict__ k_, const __hip_bfloat16* __restrict__ v_,
        const __hip_bfloat16* __restrict__ wq_, const __hip_bfloat16* __restrict__ wo_, const __hip_bfloat16* __restrict__ w1_,
        const __hip_bfloat16* __restrict__ w3_, const __hip_bfloat16* __restrict__ w2_, unsigned char* ws_,
        __hip_bfloat16* __restrict__ out_) {
  __shared__ __attribute__((aligned(16))) unsigned char lds[LDS_BYTES];
  LAS unsigned char* L = (LAS unsigned char*)lds;
  LAS unsigned* cnt = (LAS unsigned*)(L + O_CNT);
  GAS unsigned char* ws = (GAS unsigned char*)ws_;
  STAMP(0);
  const u64 t_entry = __builtin_amdgcn_s_memrealtime();
  const int bid = blockIdx.x, kvh = bid & 7, j = bid >> 3, b = j >> 4, sp = j & 15;
  const int tid = threadIdx.x, l = tid & 63, w = __builtin_amdgcn_readfirstlane(tid >> 6);
  const int g = l >> 4, qq = (l >> 2) & 3, p4 = l & 3;
  const int cw = kvh * 16 + sp;
  const int myin = WS_IN + bid * IN_STR;
  const __amdgpu_buffer_rsrc_t rs = __builtin_amdgcn_make_buffer_rsrc((void*)ws_, 0, WS_END, 0x00020000);
  const GAS unsigned char* x = (const GAS unsigned char*)x_;
  if (w < 4) {
    const size_t kvbase = ((size_t)(b * HKV + kvh) * SK + sp * KEYS_WG) * 256;
    const __amdgpu_buffer_rsrc_t rk = __builtin_amdgcn_make_buffer_rsrc((void*)((const unsigned char*)k_ + kvbase), 0, KEYS_WG * 256, 0x00020000);
    const __amdgpu_buffer_rsrc_t rv = __builtin_amdgcn_make_buffer_rsrc((void*)((const unsigned char*)v_ + kvbase), 0, KEYS_WG * 256, 0x00020000);
    const int koff = (qq * 16 + g * 4 + p4) * 16, voff = l * 16;
    u32x4 K[2][8], V[2][8];
    ST16((u32x4{INVAL, INVAL, INVAL, INVAL}), myin + w * 2048 + l * 16);
    ST16((u32x4{INVAL, INVAL, INVAL, INVAL}), w < 3 ? myin + w * 2048 + 1024 + l * 16 : (j < 28 && l < 2) ? WS_HB + (kvh * 28 + j) * 32 + l * 16 : 0x7ff00000);
    if (tid < 2) *(LAS u32x4*)(cnt + 4 * tid) = u32x4{0u, 0u, 0u, 0u};
    cfence();
    wg_barrier();
#pragma unroll
    for (int t = 0; t < 8; ++t) K[0][t] = __builtin_amdgcn_raw_buffer_load_b128(rk, koff + t * 1024, w * 8192, AUX_NT);
    cfence();
    STAMP(1);
    asm volatile("s_waitcnt vmcnt(8)" ::: "memory");
    lds_arrive(cnt + 4, l);
    lds_wait_cnt(cnt + 2, 1);
#pragma unroll
    for (int t = 0; t < 8; ++t) V[0][t] = __builtin_amdgcn_raw_buffer_load_b128(rv, voff + t * 1024, w * 8192, AUX_NT);
#pragma unroll
    for (int t = 0; t < 8; ++t) K[1][t] = __builtin_amdgcn_raw_buffer_load_b128(rk, koff + t * 1024, (4 + w) * 8192, AUX_NT);
    cfence();
    STAMP(2);
    lds_wait_cnt(cnt + 3, 4);
#pragma unroll
    for (int t = 0; t < 8; ++t) V[1][t] = __builtin_amdgcn_raw_buffer_load_b128(rv, voff + t * 1024, (4 + w) * 8192, AUX_NT);
    cfence();
    STAMP(3);
    lds_wait_cnt(cnt + 1, 4);
    STAMP(7);
    const u32x4 qf = *(LAS const u32x4*)(L + O_Q + (p4 * 128 + (4 * g + qq) * 8) * 2);
    f32x4 acc[8];
#pragma unroll
    for (int e = 0; e < 8; ++e) acc[e] = f32x4{0, 0, 0, 0};
    float m = -INFINITY, lsum = 0.f;
    int so = (8 + w) * 8192;
#pragma unroll 1
    for (int it = 0; it < NTILE / 2; ++it) {
#pragma unroll
      for (int c = 0; c < 2; ++c) {
        f32x4 d[8];
#pragma unroll
        for (int t = 0; t < 8; ++t) d[t] = mfma16(K[c][t], qf, f32x4{0, 0, 0, 0});
        cfence();
#pragma unroll
        for (int t = 0; t < 8; ++t) K[c][t] = __builtin_amdgcn_raw_buffer_load_b128(rk, koff + t * 1024, so, AUX_NT);
        cfence();
        const float S0 = qk_reduce(d[0], d[1], d[2], d[3]), S1 = qk_reduce(d[4], d[5], d[6], d[7]);
        unsigned pk = pack2(S0, S1);
        pk = pack2(blo(pk) * SCALE, bhi(pk) * SCALE);
        const float a0 = blo(pk), a1 = bhi(pk);
        float tm = fmaxf(a0, a1);
        tm = fmaxf(tm, dppf<ROR4>(tm)); tm = fmaxf(tm, dppf<ROR8>(tm)); tm = xrow_max(tm);
        if (__any(tm > m)) {
          const float mn = fmaxf(m, tm);
          const float al = __builtin_amdgcn_exp2f((m - mn) * LOG2E);
          m = mn; lsum *= al;
          const float al0 = dppf<0x00>(al), al1 = dppf<0x55>(al), al2 = dppf<0xAA>(al), al3 = dppf<0xFF>(al);
#pragma unroll
          for (int e = 0; e < 8; ++e) { acc[e][0] *= al0; acc[e][1] *= al1; acc[e][2] *= al2; acc[e][3] *= al3; }
        }
        const float p0 = __builtin_amdgcn_exp2f((a0 - m) * LOG2E), p1 = __builtin_amdgcn_exp2f((a1 - m) * LOG2E);
        lsum += p0 + p1;
        const unsigned pw = pack2(p0, p1);
        const u32x4 pf = {pw, dppu<ROR12>(pw), dppu<ROR8>(pw), dppu<ROR4>(pw)};
#pragma unroll
        for (int e = 0; e < 8; ++e) {
          const int r = e >> 1;
          const unsigned sel = (e & 1) ? 0x07060302u : 0x05040100u;
          const u32x4 bf = {__builtin_amdgcn_perm(V[c][4][r], V[c][0][r], sel), __builtin_amdgcn_perm(V[c][5][r], V[c][1][r], sel),
                            __builtin_amdgcn_perm(V[c][6][r], V[c][2][r], sel), __builtin_amdgcn_perm(V[c][7][r], V[c][3][r], sel)};
          acc[e] = mfma16(pf, bf, acc[e]);
        }
        cfence();
#pragma unroll
        for (int t = 0; t < 8; ++t) V[c][t] = __builtin_amdgcn_raw_buffer_load_b128(rv, voff + t * 1024, so, AUX_NT);
        cfence();
        so += 4 * 8192;
      }
    }
    STAMP(8);
    lsum += dppf<ROR4>(lsum); lsum += dppf<ROR8>(lsum); lsum = xrow_sum(lsum);
    LAS float* rec = (LAS float*)(L + O_REC) + w * 512;
    LAS float* ml = (LAS float*)(L + O_MLW);
    if (l < 16) {
#pragma unroll
      for (int hh = 0; hh < 4; ++hh) {
        *(LAS f32x4*)(rec + hh * 128 + 8 * l) = f32x4{acc[0][hh], acc[1][hh], acc[2][hh], acc[3][hh]};
        *(LAS f32x4*)(rec + hh * 128 + 8 * l + 4) = f32x4{acc[4][hh], acc[5][hh], acc[6][hh], acc[7][hh]};
      }
      if (l < 4) { ml[w * 8 + l * 2] = m; ml[w * 8 + l * 2 + 1] = lsum; }
    }
  } else {
    const int hw = w - 4;
    __builtin_amdgcn_s_setprio(3);
    u32x4 x0 = *(const GAS u32x4*)(x + l * 16), x1 = *(const GAS u32x4*)(x + 1024 + l * 16);
    u32x2 xw = *(const GAS u32x2*)(x + (l >> 5) * 1024 + (128 * hw + 4 * (l & 31)) * 2);
    cfence();
    u32x4 wqr[4];
#pragma unroll
    for (int i = 0; i < 4; ++i) {
      const int row = 128 * hw + 32 * i + (l >> 1);
      wqr[i] = ldnt((const GAS unsigned char*)wq_ + ((size_t)row * 4096 + kvh * 512 + j * 16 + (l & 1) * 8) * 2);
    }
    u64 E = ld_sc1((const GAS u64*)(ws + WS_EPOCH));
    cfence();
    asm volatile("s_barrier" ::: "memory");
    STAMP(1);
    asm volatile("" : "+v"(x0), "+v"(x1), "+v"(xw));
    {
      float s0 = 0.f, s1 = 0.f;
#pragma unroll
      for (int i = 0; i < 4; ++i) { s0 += blo(x0[i]) + bhi(x0[i]); s1 += blo(x1[i]) + bhi(x1[i]); }
      const float mean0 = wave_sum(s0) * (1.0f / DM), mean1 = wave_sum(s1) * (1.0f / DM);
      float q0 = 0.f, q1 = 0.f;
#pragma unroll
      for (int i = 0; i < 4; ++i) {
        float d;
        d = blo(x0[i]) - mean0; q0 += d * d; d = bhi(x0[i]) - mean0; q0 += d * d;
        d = blo(x1[i]) - mean1; q1 += d * d; d = bhi(x1[i]) - mean1; q1 += d * d;
      }
      const float var0 = wave_sum(q0) * (1.0f / DM), var1 = wave_sum(q1) * (1.0f / DM);
      const float mb = rbf(l < 32 ? mean0 : mean1);
      const float den = rbf(sqrtf(rbf(rbf(l < 32 ? var0 : var1) + 1e-5f)));
      *(LAS u32x2*)(L + O_XN + (l >> 5) * 1024 + (128 * hw + 4 * (l & 31)) * 2) =
          u32x2{pack2(rbf(blo(xw[0]) - mb) / den, rbf(bhi(xw[0]) - mb) / den), pack2(rbf(blo(xw[1]) - mb) / den, rbf(bhi(xw[1]) - mb) / den)};
    }
    STAMP(2);
#pragma unroll
    for (int i = 0; i < 4; ++i) *(LAS u32x4*)(L + O_WQ + hw * 4096 + i * 1024 + l * 16) = wqr[i];
    lds_wait();
    STAMP(3);
    LAS float* part = (LAS float*)(L + O_PART);
    {
      f32x4 dq = {0, 0, 0, 0};
#pragma unroll
      for (int s = 0; s < 4; ++s) {
        const u32x4 af = *(LAS const u32x4*)(L + O_XN + (l & 1) * 1024 + (128 * hw + 32 * s + 8 * g) * 2);
        const int ro = (32 * s + 8 * g + qq) * 32 + p4 * 8;
        const u32x2 b0 = tr_read(L + O_WQ + hw * 4096 + ro), b1 = tr_read(L + O_WQ + hw * 4096 + ro + 128);
        dq = mfma16(af, u32x4{b0[0], b0[1], b1[0], b1[1]}, dq);
      }
      if (l < 16) { part[hw * 32 + l] = dq[0]; part[hw * 32 + 16 + l] = dq[1]; }
    }
    lds_arrive(cnt, l);
    asm volatile("" : "+v"(E));
    const u64 T = E + 1;
    STAMP(4);
    if (hw == 0) {
      lds_wait_cnt(cnt, 4);
      if (l < 32) {
        const float s = (part[l] + part[32 + l]) + (part[64 + l] + part[96 + l]);
        st_sc1((GAS u64*)(ws + WS_QBOX) + ((l >> 4) * 8 + kvh) * 512 + j * 16 + (l & 15), (T << 16) | (pack2(s, s) & 0xffffu));
      }
      cfence();
      lds_arrive(cnt + 2, l);
    } else {
      if (hw == 3) {
        lds_wait_cnt(cnt + 4, 4);
        if (l == 0) st_sc1((GAS u64*)(ws + WS_FLAG) + bid, T);
      }
    }
    STAMP(5);
    while (__builtin_amdgcn_s_memrealtime() - t_entry < QPOLL_T) __builtin_amdgcn_s_sleep(2);
    const bool ffn = j < 28 && !NO_FFN;
    const int fs = kvh * 28 + (ffn ? j : 0);
    const int rb = fs & 15, cb = fs >> 4;
    const int qoff = WS_QBOX + ((b * 8 + kvh) * 512 + (tid - 256) * 2) * 8;
    const unsigned thi = (unsigned)(T >> 16), tlo = (unsigned)T & 0xffffu;
#define Q_FINISH() do { \
      asm volatile("" : "+v"(qg)); \
      unsigned spins = 0; \
      while (qg[1] != thi || qg[3] != thi || (qg[0] >> 16) != tlo || (qg[2] >> 16) != tlo) { \
        if (++spins > SPIN_CAP) break; \
        DIAG_RETRY; \
        asm volatile("" ::: "memory"); \
        qg = LD16(qoff); \
      } \
      *(LAS unsigned*)(L + O_Q + (tid - 256) * 4) = (qg[0] & 0xffffu) | (qg[2] << 16); \
      lds_arrive(cnt + 1, l); \
      STAMP(6); } while (0)
    if (hw != 0) {
      u32x4 qg = LD16(qoff);
      cfence();
      lds_arrive(cnt + 3, l);
      Q_FINISH();
    }
    if (hw == 0) {
      u32x4 qg = LD16(qoff);
      cfence();
      lds_arrive(cnt + 3, l);
      u32x4 wa[8], wb[8], w2r[7];
      {
        const size_t wofs = ((size_t)(rb * 32 + 8 * g) * FF + cb * 128 + (l & 15) * 8) * 2;
#pragma unroll
        for (int t = 0; t < 8; ++t) wa[t] = ldnt((const GAS unsigned char*)w1_ + wofs + (size_t)t * FF * 2);
#pragma unroll
        for (int t = 0; t < 8; ++t) wb[t] = ldnt((const GAS unsigned char*)w3_ + wofs + (size_t)t * FF * 2);
      }
#pragma unroll
      for (int i = 0; i < 7; ++i) w2r[i] = ldnt((const GAS unsigned char*)w2_ + ((size_t)(56 * j + 8 * i + (l >> 3)) * 512 + 64 * kvh + (l & 7) * 8) * 2);
      u32x4 f0 = LD16(WS_FLAG + l * 32), f1 = LD16(WS_FLAG + l * 32 + 16);
      cfence();
      Q_FINISH();
      {
        const unsigned tl = (unsigned)T, th = (unsigned)(T >> 32);
        unsigned spins = 0;
        asm volatile("" : "+v"(f0), "+v"(f1));
        for (;;) {
          const bool ok = f0[0] == tl && f0[1] == th && f0[2] == tl && f0[3] == th && f1[0] == tl && f1[1] == th && f1[2] == tl && f1[3] == th;
          if (__all(ok) || ++spins > SPIN_CAP) break;
          asm volatile("" ::: "memory");
          f0 = LD16(WS_FLAG + l * 32); f1 = LD16(WS_FLAG + l * 32 + 16);
        }
        cfence();
      }
      if (bid == 0 && l == 0) st_sc1((GAS u64*)(ws + WS_EPOCH), T);
      STAMP(7);
#pragma unroll
      for (int i = 0; i < 7; ++i) *(LAS u32x4*)(L + O_W2 + (8 * i + (l >> 3)) * W2_STR + (l & 7) * 16) = w2r[i];
      if (ffn) {
        const u32x4 xf = *(LAS const u32x4*)(L + O_XN + (l & 1) * 1024 + (rb * 32 + 8 * g) * 2);
        f32x4 r[8];
#pragma unroll
        for (int e = 0; e < 8; ++e) {
          const int q2 = e >> 1;
          const unsigned sel = (e & 1) ? 0x07060302u : 0x05040100u;
          const u32x4 fa = {__builtin_amdgcn_perm(wa[1][q2], wa[0][q2], sel), __builtin_amdgcn_perm(wa[3][q2], wa[2][q2], sel),
                            __builtin_amdgcn_perm(wa[5][q2], wa[4][q2], sel), __builtin_amdgcn_perm(wa[7][q2], wa[6][q2], sel)};
          const u32x4 fb = {__builtin_amdgcn_perm(wb[1][q2], wb[0][q2], sel), __builtin_amdgcn_perm(wb[3][q2], wb[2][q2], sel),
                            __builtin_amdgcn_perm(wb[5][q2], wb[4][q2], sel), __builtin_amdgcn_perm(wb[7][q2], wb[6][q2], sel)};
          const f32x4 da = mfma16(xf, fa, f32x4{0, 0, 0, 0}), db = mfma16(xf, fb, f32x4{0, 0, 0, 0});
          r[0 + (e >> 2)][e & 3] = da[0]; r[2 + (e >> 2)][e & 3] = da[1];
          r[4 + (e >> 2)][e & 3] = db[0]; r[6 + (e >> 2)][e & 3] = db[1];
        }
        if (l < 16) {
#pragma unroll
          for (int i = 0; i < 8; ++i) *(LAS f32x4*)(L + O_GU + l * 144 + i * 16) = r[i];
        }
        lds_wait();
#pragma unroll
        for (int i = 0; i < 2; ++i)
          ST16(san4(*(LAS f32x4*)(L + O_GU + (8 * i + (l >> 3)) * 144 + (l & 7) * 16)), myin + IN_GU + i * 1024 + l * 16);
        const int pf0 = cb * 16 + (l >> 3), pf1 = pf0 + 8;
        const int po0 = WS_IN + (((pf0 % 28) << 3) | (pf0 / 28)) * IN_STR + IN_GU + rb * 128 + (l & 7) * 16;
        const int po1 = WS_IN + (((pf1 % 28) << 3) | (pf1 / 28)) * IN_STR + IN_GU + rb * 128 + (l & 7) * 16;
        u32x4 g0, g1;
        unsigned spins = 0;
        for (;;) {
          asm volatile("" ::: "memory");
          g0 = LD16(po0); g1 = LD16(po1);
          if (__all(ok4(g0) && ok4(g1)) || ++spins > SPIN_CAP) break;
          __builtin_amdgcn_s_sleep(8);
        }
        STAMP(13);
        float hv[4];
#pragma unroll
        for (int n = 0; n < 4; ++n) {
          float t = asf(g0[n]) + asf(g1[n]); t += dppf<ROR8>(t); t = rbf(xrow_sum(t));
          hv[n] = rbf(silu(t)) * dppf<ROR12>(t);
        }
        u32x4 hp;
#pragma unroll
        for (int n = 0; n < 4; ++n) { const unsigned u = pack2(hv[n], dppf<XOR2>(hv[n])); hp[n] = u == INVAL ? 0x7FC07FC0u : u; }
        if (l < 2) ST16(hp, WS_HB + fs * 32 + l * 16);
      }
      {
        u32x4 hg;
        unsigned spins = 0;
        for (;;) {
          asm volatile("" ::: "memory");
          hg = LD16(l < 14 ? WS_HB + j * 224 + l * 16 : 0x7ff00000);
          if (NO_FFN || __all(ok4(hg)) || ++spins > SPIN_CAP) break;
          __builtin_amdgcn_s_sleep(8);
        }
        STAMP(14);
        if (l < 16) {
          *(LAS u32x2*)(L + O_H + l * 8) = u32x2{__builtin_amdgcn_perm(hg[1], hg[0], 0x05040100u), __builtin_amdgcn_perm(hg[3], hg[2], 0x05040100u)};
          *(LAS u32x2*)(L + O_H + 128 + l * 8) = u32x2{__builtin_amdgcn_perm(hg[1], hg[0], 0x07060302u), __builtin_amdgcn_perm(hg[3], hg[2], 0x07060302u)};
        }
        lds_wait();
        f32x4 o0, o1;
#pragma unroll
        for (int n = 0; n < 4; ++n) {
          f32x4 d = {0, 0, 0, 0};
#pragma unroll
          for (int ks = 0; ks < 2; ++ks) {
            const u32x4 af = *(LAS const u32x4*)(L + O_H + (l & 1) * 128 + (32 * ks + 8 * g) * 2);
            const int r0 = min(32 * ks + 8 * g + qq, 51);
            const int ro = r0 * W2_STR + (16 * n + 4 * p4) * 2;
            const u32x2 b0 = tr_read(L + O_W2 + ro), b1 = tr_read(L + O_W2 + ro + 4 * W2_STR);
            d = mfma16(af, u32x4{b0[0], b0[1], b1[0], b1[1]}, d);
          }
          o0[n] = d[0]; o1[n] = d[1];
        }
        f32x4 ob;
#pragma unroll
        for (int n = 0; n < 4; ++n) { auto t = __builtin_amdgcn_permlane16_swap(asu(o0[n]), asu(o1[n]), false, false); ob[n] = asf(t[0]); }
        if (l < 32) ST16(san4(ob), myin + IN_F2 + l * 16);
      }
      lds_arrive(cnt + 2, l);
      STAMP(8);
    } else if (hw == 1) {
      const GAS unsigned char* wb = (const GAS unsigned char*)wo_ + ((size_t)(kvh * 512 + 32 * sp) * 512 + l * 8) * 2;
#pragma unroll 1
      for (int h = 0; h < (NO_WO ? 0 : 2); ++h) {
        u32x4 wr[16];
#pragma unroll
        for (int i = 0; i < 16; ++i) wr[i] = ldnt(wb + (size_t)(h * 16 + i) * 1024);
        cfence();
#pragma unroll
        for (int i = 0; i < 16; ++i) *(LAS u32x4*)(L + O_WO + (h * 16 + i) * WO_STR + l * 16) = wr[i];
      }
      STAMP(8);
    } else if (hw == 2) {
      while (__builtin_amdgcn_readfirstlane(__hip_atomic_load(cnt + 2, __ATOMIC_RELAXED, __HIP_MEMORY_SCOPE_WORKGROUP)) < 2) __builtin_amdgcn_s_sleep(32);
      asm volatile("" ::: "memory");
      u32x4 fp;
      unsigned spins = 0;
      for (;;) {
        asm volatile("" ::: "memory");
        fp = LD16(l < 32 ? WS_IN + ((l << 3) | kvh) * IN_STR + IN_F2 + (b * 16 + sp) * 16 : 0x7ff00000);
        if (__all(ok4(fp)) || ++spins > SPIN_CAP) break;
        __builtin_amdgcn_s_sleep(16);
      }
      float f[4];
#pragma unroll
      for (int n = 0; n < 4; ++n) f[n] = wave_sum(asf(fp[n]));
      const float fv = l == 0 ? f[0] : l == 1 ? f[1] : l == 2 ? f[2] : f[3];
      if (l < 4) ((LAS float*)(L + O_FG))[l] = rbf(silu(rbf(fv)));
      STAMP(8);
    }
    __builtin_amdgcn_s_setprio(0);
  }
  const float xv = (float)((const GAS __bf16*)x_)[b * 512 + 64 * (cw >> 4) + 16 * (l & 3) + (cw & 15)];
  wg_barrier();
  STAMP(9);
  if (w < 2) {
    LAS float* ml = (LAS float*)(L + O_MLW);
    const int hh = tid >> 5;
    float M = -INFINITY;
#pragma unroll
    for (int ww = 0; ww < 4; ++ww) M = fmaxf(M, ml[ww * 8 + hh * 2]);
    f32x4 o = {0, 0, 0, 0};
    float Ls = 0.f;
#pragma unroll
    for (int ww = 0; ww < 4; ++ww) {
      const float f = __builtin_amdgcn_exp2f((ml[ww * 8 + hh * 2] - M) * LOG2E);
      o += f * *(LAS f32x4*)(L + O_REC + ww * 2048 + tid * 16);
      Ls += f * ml[ww * 8 + hh * 2 + 1];
    }
    const int cin = WS_IN + (((b * 16 + (tid >> 3)) << 3) | kvh) * IN_STR;
    ST16(san4(o), cin + IN_H1 + sp * 128 + (tid & 7) * 16);
    if ((tid & 7) == 0) ST8((u32x2{san(M), san(Ls)}), cin + IN_ML + sp * 8);
  }
  STAMP(10);
  u32x4 wf[4];
#pragma unroll
  for (int n = 0; n < 4; ++n) {
    const int ro = (8 * g + qq) * WO_STR + (64 * w + 16 * n + 4 * p4) * 2;
    const u32x2 b0 = tr_read(L + O_WO + ro), b1 = tr_read(L + O_WO + ro + 4 * WO_STR);
    wf[n] = u32x4{b0[0], b0[1], b1[0], b1[1]};
  }
  if (w == 0) {
    u32x4 o0, o1;
    u32x2 mlg;
    unsigned spins = 0;
    for (;;) {
      asm volatile("" ::: "memory");
      o0 = LD16(myin + IN_H1 + l * 16); o1 = LD16(myin + IN_H1 + 1024 + l * 16);
      mlg = LD8(myin + IN_ML + (l & 15) * 8);
      if (__all(ok4(o0) && ok4(o1) && mlg[0] != INVAL && mlg[1] != INVAL) || ++spins > SPIN_CAP) break;
      __builtin_amdgcn_s_sleep(TSLEEP);
    }
    STAMP(11);
    const float mp = asf(mlg[0]), lp = asf(mlg[1]);
    float M = mp;
    M = fmaxf(M, dppf<XOR1>(M)); M = fmaxf(M, dppf<XOR2>(M)); M = fmaxf(M, dppf<ROR4>(M)); M = fmaxf(M, dppf<ROR8>(M));
    const float f = __builtin_amdgcn_exp2f((mp - M) * LOG2E);
    float Ls = f * lp;
    Ls += dppf<XOR1>(Ls); Ls += dppf<XOR2>(Ls); Ls += dppf<ROR4>(Ls); Ls += dppf<ROR8>(Ls);
    const float f0 = __shfl(f, l >> 3), f1 = __shfl(f, 8 + (l >> 3));
    float ov[4];
#pragma unroll
    for (int n = 0; n < 4; ++n) { float t = asf(o0[n]) * f0 + asf(o1[n]) * f1; t += dppf<ROR8>(t); ov[n] = xrow_sum(t) / Ls; }
    if (l < 8) *(LAS u32x2*)(L + O_O + l * 8) = u32x2{pack2(ov[0], ov[1]), pack2(ov[2], ov[3])};
  }
  wg_barrier();
  {
    const u32x4 af = *(LAS const u32x4*)(L + O_O + g * 16);
    f32x4 so;
#pragma unroll
    for (int n = 0; n < 4; ++n) so[n] = mfma16(af, wf[n], f32x4{0, 0, 0, 0})[0];
    if (l < 16) ST16(san4(so), myin + IN_H2 + (16 * w + l) * 16);
  }
  STAMP(12);
  if (w == 0) {
    u32x4 s0, s1;
    unsigned spins = 0;
    for (;;) {
      asm volatile("" ::: "memory");
      s0 = LD16(WS_IN + (b * 128 + l) * IN_STR + IN_H2 + cw * 16); s1 = LD16(WS_IN + (b * 128 + 64 + l) * IN_STR + IN_H2 + cw * 16);
      if (__all(ok4(s0) && ok4(s1)) || ++spins > SPIN_CAP) break;
      __builtin_amdgcn_s_sleep(TSLEEP);
    }
    STAMP(13);
    float f[4];
#pragma unroll
    for (int n = 0; n < 4; ++n) f[n] = wave_sum(asf(s0[n]) + asf(s1[n]));
    const float s = l == 0 ? f[0] : l == 1 ? f[1] : l == 2 ? f[2] : f[3];
    if (l < 4) ((GAS __bf16*)out_)[b * 512 + 64 * (cw >> 4) + 16 * l + (cw & 15)] = (__bf16)(xv + rbf(rbf(s) * ((LAS float*)(L + O_FG))[l]));
  }
  if (w != 4) STAMP(14);
}

extern "C" void kernel_launch(void* const* d_in, const int* in_sizes, int n_in, void* d_out, int out_size, void* d_ws, size_t ws_size,
                              hipStream_t stream) {
  k_fused<<<NWG, NTHR, 0, stream>>>((const __hip_bfloat16*)d_in[0], (const __hip_bfloat16*)d_in[1], (const __hip_bfloat16*)d_in[2],
                                    (const __hip_bfloat16*)d_in[3], (const __hip_bfloat16*)d_in[4], (const __hip_bfloat16*)d_in[6],
                                    (const __hip_bfloat16*)d_in[7], (const __hip_bfloat16*)d_in[8], (unsigned char*)d_ws,
                                    (__hip_bfloat16*)d_out);
}
```

```cpp
#include <hip/hip_runtime.h>
#include <hip/hip_bf16.h>
#include <stdint.h>

#define DIAG 0
#define QPOLL_T 460
#define TSLEEP 2
#define F2_EARLY 0
#define PACE_D 220
#define PACE_0 650
#define N1 13
#define T_C 2200
#define RFAST 1.04f
#define NT_WO 1

#define GAS __attribute__((address_space(1)))
#define LAS __attribute__((address_space(3)))
typedef unsigned u32x4 __attribute__((ext_vector_type(4)));
typedef unsigned u32x2 __attribute__((ext_vector_type(2)));
typedef float f32x4 __attribute__((ext_vector_type(4)));
typedef short s16x8 __attribute__((ext_vector_type(8)));
typedef short s16x4 __attribute__((ext_vector_type(4)));
typedef __bf16 bf16x2_t __attribute__((ext_vector_type(2)));
typedef unsigned long long u64;
typedef GAS u64 gu64;

constexpr int DM = 512, HKV = 8, SK = 32768, FF = 1792;
constexpr int NWG = 256, NTHR = 512, NSPLIT = 16;
constexpr int KEYS_WG = SK / NSPLIT;
constexpr int NTILE = KEYS_WG / (32 * 4);
constexpr int NFFP = FF / 8;
constexpr float SCALE = 0.08838834764831845f;
constexpr float LOG2E = 1.4426950408889634f;
constexpr unsigned SPIN_CAP = 1u << 15;
constexpr unsigned INVAL = 0xFFFFFFFFu;
constexpr int OOB = 0x7ff00000;

constexpr int WS_EPOCH = 0;
constexpr int WS_FLAG = 2048;
constexpr int WS_PROG = 4096;
constexpr int WS_QBOX = 8192;
constexpr int WS_HB = WS_QBOX + 65536;
constexpr int WS_IN = WS_HB + 8192;
constexpr int IN_H1 = 0;
constexpr int IN_H2 = 2048;
constexpr int IN_GU = 4096;
constexpr int IN_F2 = 6144;
constexpr int IN_ML = 6656;
constexpr int IN_STR = 7168;
constexpr int WS_END = WS_IN + NWG * IN_STR;
constexpr size_t WS_DIAG = WS_END;

constexpr int WO_STR = 1056;
constexpr int O_XN = 0;
constexpr int O_WQ = 2048;
constexpr int W2_STR = 160;
constexpr int O_W2 = O_WQ + 16384;
constexpr int O_GU = O_W2 + 56 * W2_STR + 64;
constexpr int O_PART = O_GU + 2 * 16 * 80;
constexpr int O_H = O_PART + 512;
constexpr int O_Q = O_H + 256;
constexpr int O_WO = O_Q + 1024;
constexpr int O_REC = O_WO + 32 * WO_STR;
constexpr int O_F = O_REC + 8192;
constexpr int O_MLW = O_F + 512;
constexpr int O_O = O_MLW + 128;
constexpr int O_FG = O_O + 64;
constexpr int O_CNT = O_FG + 16;
constexpr int LDS_BYTES = 84 * 1024;
static_assert(O_CNT + 48 <= LDS_BYTES, "");
constexpr int PPM = 64 - 4 * N1;

#define RLX_AGENT __ATOMIC_RELAXED, __HIP_MEMORY_SCOPE_AGENT
__device__ __forceinline__ float asf(unsigned u) { return __builtin_bit_cast(float, u); }
__device__ __forceinline__ unsigned asu(float f) { return __builtin_bit_cast(unsigned, f); }
__device__ __forceinline__ unsigned san(float f) { const unsigned u = asu(f); return u == INVAL ? 0x7FC00000u : u; }
__device__ __forceinline__ u32x4 san4(f32x4 v) { return u32x4{san(v[0]), san(v[1]), san(v[2]), san(v[3])}; }
__device__ __forceinline__ bool ok4(u32x4 v) { return v[0] != INVAL && v[1] != INVAL && v[2] != INVAL && v[3] != INVAL; }
__device__ __forceinline__ unsigned pack2(float lo, float hi) {
  bf16x2_t v = {(__bf16)lo, (__bf16)hi};
  return __builtin_bit_cast(unsigned, v);
}
__device__ __forceinline__ float rbf(float x) { return (float)(__bf16)x; }
__device__ __forceinline__ float blo(unsigned u) { return asf(u << 16); }
__device__ __forceinline__ float bhi(unsigned u) { return asf(u & 0xffff0000u); }
__device__ __forceinline__ f32x4 mfma16(u32x4 a, u32x4 b, f32x4 c) {
  return __builtin_amdgcn_mfma_f32_16x16x32_bf16(__builtin_bit_cast(s16x8, a), __builtin_bit_cast(s16x8, b), c, 0, 0, 0);
}
template <int CTRL> __device__ __forceinline__ float dppf(float x) {
  return asf(__builtin_amdgcn_mov_dpp(asu(x), CTRL, 0xf, 0xf, true));
}
template <int CTRL> __device__ __forceinline__ unsigned dppu(unsigned x) {
  return __builtin_amdgcn_mov_dpp(x, CTRL, 0xf, 0xf, true);
}
template <int CTRL, int BANK> __device__ __forceinline__ float updf(float old, float x) {
  return asf(__builtin_amdgcn_update_dpp(asu(old), asu(x), CTRL, 0xf, BANK, false));
}
constexpr int ROR4 = 0x124, ROR8 = 0x128, ROR12 = 0x12C, QID = 0xE4, XOR1 = 0xB1, XOR2 = 0x4E;
__device__ __forceinline__ float xrow_max(float x) {
  auto s = __builtin_amdgcn_permlane16_swap(asu(x), asu(x), false, false);
  x = fmaxf(asf(s[0]), asf(s[1]));
  auto t = __builtin_amdgcn_permlane32_swap(asu(x), asu(x), false, false);
  return fmaxf(asf(t[0]), asf(t[1]));
}
__device__ __forceinline__ float xrow_sum(float x) {
  auto s = __builtin_amdgcn_permlane16_swap(asu(x), asu(x), false, false);
  x = asf(s[0]) + asf(s[1]);
  auto t = __builtin_amdgcn_permlane32_swap(asu(x), asu(x), false, false);
  return asf(t[0]) + asf(t[1]);
}
__device__ __forceinline__ float wave_sum(float x) {
  x += dppf<XOR1>(x); x += dppf<XOR2>(x); x += dppf<ROR4>(x); x += dppf<ROR8>(x);
  return xrow_sum(x);
}
__device__ __forceinline__ u32x2 tr_read(LAS const unsigned char* p) {
  return __builtin_bit_cast(u32x2, __builtin_amdgcn_ds_read_tr16_b64_v4i16((LAS s16x4*)p));
}
__device__ __forceinline__ float silu(float x) { return x / (1.0f + __expf(-x)); }
__device__ __forceinline__ void wg_barrier() { asm volatile("s_waitcnt lgkmcnt(0)\n\ts_barrier" ::: "memory"); }
__device__ __forceinline__ void lds_wait() { asm volatile("s_waitcnt lgkmcnt(0)" ::: "memory"); }
__device__ __forceinline__ void cfence() { asm volatile("" ::: "memory"); __builtin_amdgcn_sched_barrier(0); }
__device__ __forceinline__ u32x4 ldnt(const GAS unsigned char* p) { return __builtin_nontemporal_load((const GAS u32x4*)p); }
template <int NT> __device__ __forceinline__ u32x4 ldw(const GAS unsigned char* p) {
  if constexpr (NT) return __builtin_nontemporal_load((const GAS u32x4*)p); else return *(const GAS u32x4*)p;
}
__device__ __forceinline__ u64 ld_sc1(const GAS u64* p) { return __hip_atomic_load((gu64*)p, RLX_AGENT); }
__device__ __forceinline__ void st_sc1(GAS u64* p, u64 v) { __hip_atomic_store(p, v, RLX_AGENT); }
constexpr int AUX_SC1 = 16, AUX_NT = 2;
#define LD16(off) __builtin_amdgcn_raw_buffer_load_b128(rs, (off), 0, AUX_SC1)
#define LD8(off) __builtin_amdgcn_raw_buffer_load_b64(rs, (off), 0, AUX_SC1)
#define ST16(v, off) __builtin_amdgcn_raw_buffer_store_b128((v), rs, (off), 0, AUX_SC1)
#define ST8(v, off) __builtin_amdgcn_raw_buffer_store_b64((v), rs, (off), 0, AUX_SC1)
__device__ __forceinline__ void lds_arrive(LAS unsigned* c, int l) {
  asm volatile("s_waitcnt lgkmcnt(0)" ::: "memory");
  if (l == 0) __hip_atomic_fetch_add(c, 1u, __ATOMIC_RELAXED, __HIP_MEMORY_SCOPE_WORKGROUP);
}
__device__ __forceinline__ void lds_wait_cnt(LAS unsigned* c, unsigned n) {
  while (__builtin_amdgcn_readfirstlane(__hip_atomic_load(c, __ATOMIC_RELAXED, __HIP_MEMORY_SCOPE_WORKGROUP)) < n) __builtin_amdgcn_s_sleep(1);
  asm volatile("" ::: "memory");
}
__device__ __forceinline__ int ffn_wg(int fs) { return ((fs % 28) << 3) | (fs / 28); }

__device__ __forceinline__ float qk_reduce(const f32x4 d0, const f32x4 d1, const f32x4 d2, const f32x4 d3) {
  float z0 = d0[0];
  z0 = updf<QID, 0x2>(z0, d1[1]); z0 = updf<QID, 0x4>(z0, d2[2]); z0 = updf<QID, 0x8>(z0, d3[3]);
  float z1 = dppf<ROR4>(d0[3]);
  z1 = updf<ROR4, 0x2>(z1, d1[0]); z1 = updf<ROR4, 0x4>(z1, d2[1]); z1 = updf<ROR4, 0x8>(z1, d3[2]);
  float z2 = dppf<ROR8>(d0[2]);
  z2 = updf<ROR8, 0x2>(z2, d1[3]); z2 = updf<ROR8, 0x4>(z2, d2[0]); z2 = updf<ROR8, 0x8>(z2, d3[1]);
  float z3 = dppf<ROR12>(d0[1]);
  z3 = updf<ROR12, 0x2>(z3, d1[2]); z3 = updf<ROR12, 0x4>(z3, d2[3]); z3 = updf<ROR12, 0x8>(z3, d3[0]);
  return (z0 + z1) + (z2 + z3);
}

#define DIAG_RETRY
#define STAMP(i) do { } while (0)

__global__ void __launch_bounds__(NTHR, 2)
k_fused(const __hip_bfloat16* __restrict__ x_, const __hip_bfloat16* __restrict__ k_, const __hip_bfloat16* __restrict__ v_,
        const __hip_bfloat16* __restrict__ wq_, const __hip_bfloat16* __restrict__ wo_, const __hip_bfloat16* __restrict__ w1_,
        const __hip_bfloat16* __restrict__ w3_, const __hip_bfloat16* __restrict__ w2_, unsigned char* ws_,
        __hip_bfloat16* __restrict__ out_) {
  __shared__ __attribute__((aligned(16))) unsigned char lds[LDS_BYTES];
  LAS unsigned char* L = (LAS unsigned char*)lds;
  LAS unsigned* cnt = (LAS unsigned*)(L + O_CNT);
  GAS unsigned char* ws = (GAS unsigned char*)ws_;
  STAMP(0);
  const u64 t_entry = __builtin_amdgcn_s_memrealtime();
  const int bid = blockIdx.x, kvh = bid & 7, j = bid >> 3, b = j >> 4, sp = j & 15;
  const int gid = bid >> 4, akvh = gid & 7, asp = bid & 15;
  const int tid = threadIdx.x, l = tid & 63, w = __builtin_amdgcn_readfirstlane(tid >> 6);
  const int g = l >> 4, qq = (l >> 2) & 3, p4 = l & 3;
  const int cw = kvh * 16 + sp;
  const int myin = WS_IN + bid * IN_STR;
  const bool ffn = j < 28;
  const int fs = kvh * 28 + (ffn ? j : 0);
  const int rb = fs & 15, cb = fs >> 4;
  const __amdgpu_buffer_rsrc_t rs = __builtin_amdgcn_make_buffer_rsrc((void*)ws_, 0, WS_END, 0x00020000);
  const GAS unsigned char* x = (const GAS unsigned char*)x_;
  u64 Tep = 0;
  if (w < 4) {
    const size_t kvbase = (size_t)gid * SK * 256;
    const __amdgpu_buffer_rsrc_t rk = __builtin_amdgcn_make_buffer_rsrc((void*)((const unsigned char*)k_ + kvbase), 0, SK * 256, 0x00020000);
    const __amdgpu_buffer_rsrc_t rv = __builtin_amdgcn_make_buffer_rsrc((void*)((const unsigned char*)v_ + kvbase), 0, SK * 256, 0x00020000);
    const int so0 = (asp * 64 + w) * 8192;
    const int koff = (qq * 16 + g * 4 + p4) * 16, voff = l * 16;
    u32x4 K[2][8], V[2][8];
#define LOADK(c_, so_) _Pragma("unroll") for (int t = 0; t < 8; ++t) K[c_][t] = __builtin_amdgcn_raw_buffer_load_b128(rk, koff + t * 1024, (so_), AUX_NT)
#define LOADV(c_, so_) _Pragma("unroll") for (int t = 0; t < 8; ++t) V[c_][t] = __builtin_amdgcn_raw_buffer_load_b128(rv, voff + t * 1024, (so_), AUX_NT)
    ST16((u32x4{INVAL, INVAL, INVAL, INVAL}), myin + w * 2048 + l * 16);
    ST16((u32x4{INVAL, INVAL, INVAL, INVAL}), w < 3 ? myin + w * 2048 + 1024 + l * 16 : (ffn && l < 2) ? WS_HB + fs * 32 + l * 16 : OOB);
    if (tid < 2) *(LAS u32x4*)(cnt + 4 * tid) = u32x4{0u, 0u, 0u, 0u};
    cfence();
    LOADK(0, so0);
    cfence();
    wg_barrier();
    STAMP(1);
    asm volatile("s_waitcnt vmcnt(8)" ::: "memory");
    lds_arrive(cnt + 4, l);
    lds_wait_cnt(cnt + 2, 1);
    LOADV(0, so0);
    LOADK(1, so0 + 4 * 8192);
    cfence();
    STAMP(2);
    lds_wait_cnt(cnt + 3, 4);
    LOADV(1, so0 + 4 * 8192);
    cfence();
    STAMP(3);
    lds_wait_cnt(cnt + 1, 4);
    STAMP(7);
    const u32x4 qf = *(LAS const u32x4*)(L + O_Q + (p4 * 128 + (4 * g + qq) * 8) * 2);
    f32x4 acc[8];
#pragma unroll
    for (int e = 0; e < 8; ++e) acc[e] = f32x4{0, 0, 0, 0};
    float m = -INFINITY, lsum = 0.f;
    unsigned tgo = (unsigned)t_entry + PACE_0;
#define PACE() do { if (PACE_D && nx < N1) { while ((int)((unsigned)__builtin_amdgcn_s_memrealtime() - tgo) < 0) __builtin_amdgcn_s_sleep(1); tgo += PACE_D / 2; } } while (0)
    int nx = 2;
    int pstart = 0, pcount = 0;
    bool valid[2] = {true, true};
#pragma unroll 1
    do {
#pragma unroll
      for (int c = 0; c < 2; ++c) {
        f32x4 d[8];
#pragma unroll
        for (int t = 0; t < 8; ++t) d[t] = mfma16(K[c][t], qf, f32x4{0, 0, 0, 0});
        cfence();
        const bool cur = valid[c];
        if (nx == N1) {
          lds_wait_cnt(cnt + 7, 1);
          pstart = __builtin_amdgcn_readfirstlane(cnt[8]); pcount = __builtin_amdgcn_readfirstlane(cnt[9]);
        }
        int so = so0 + nx * (4 * 8192);
        if (nx >= N1) {
          const int q = (nx - N1) * 4 + w, pi = pstart + q, m2 = pi / PPM;
          valid[c] = q < pcount;
          so = valid[c] ? (m2 * 64 + 4 * N1 + (pi - m2 * PPM)) * 8192 : OOB;
        }
        PACE();
        LOADK(c, so);
        cfence();
        if (l == 0) __hip_atomic_fetch_add(cnt + 6, 1u, __ATOMIC_RELAXED, __HIP_MEMORY_SCOPE_WORKGROUP);
        float S0 = qk_reduce(d[0], d[1], d[2], d[3]), S1 = qk_reduce(d[4], d[5], d[6], d[7]);
        if (!cur) S0 = S1 = -INFINITY;
        unsigned pk = pack2(S0, S1);
        pk = pack2(blo(pk) * SCALE, bhi(pk) * SCALE);
        const float a0 = blo(pk), a1 = bhi(pk);
        float tm = fmaxf(a0, a1);
        tm = fmaxf(tm, dppf<ROR4>(tm)); tm = fmaxf(tm, dppf<ROR8>(tm)); tm = xrow_max(tm);
        if (__any(tm > m)) {
          const float mn = fmaxf(m, tm);
          const float al = __builtin_amdgcn_exp2f((m - mn) * LOG2E);
          m = mn; lsum *= al;
          const float al0 = dppf<0x00>(al), al1 = dppf<0x55>(al), al2 = dppf<0xAA>(al), al3 = dppf<0xFF>(al);
#pragma unroll
          for (int e = 0; e < 8; ++e) { acc[e][0] *= al0; acc[e][1] *= al1; acc[e][2] *= al2; acc[e][3] *= al3; }
        }
        const float p0 = __builtin_amdgcn_exp2f((a0 - m) * LOG2E), p1 = __builtin_amdgcn_exp2f((a1 - m) * LOG2E);
        lsum += p0 + p1;
        const unsigned pw = pack2(p0, p1);
        const u32x4 pf = {pw, dppu<ROR12>(pw), dppu<ROR8>(pw), dppu<ROR4>(pw)};
#pragma unroll
        for (int e = 0; e < 8; ++e) {
          const int r = e >> 1;
          const unsigned sel = (e & 1) ? 0x07060302u : 0x05040100u;
          const u32x4 bf = {__builtin_amdgcn_perm(V[c][4][r], V[c][0][r], sel), __builtin_amdgcn_perm(V[c][5][r], V[c][1][r], sel),
                            __builtin_amdgcn_perm(V[c][6][r], V[c][2][r], sel), __builtin_amdgcn_perm(V[c][7][r], V[c][3][r], sel)};
          acc[e] = mfma16(pf, bf, acc[e]);
        }
        cfence();
        PACE();
        LOADV(c, so);
        cfence();
        if (l == 0) __hip_atomic_fetch_add(cnt + 6, 1u, __ATOMIC_RELAXED, __HIP_MEMORY_SCOPE_WORKGROUP);
        ++nx;
      }
    } while (valid[0]);
    STAMP(8);
    lsum += dppf<ROR4>(lsum); lsum += dppf<ROR8>(lsum); lsum = xrow_sum(lsum);
    LAS float* rec = (LAS float*)(L + O_REC) + w * 512;
    LAS float* ml = (LAS float*)(L + O_MLW);
    if (l < 16) {
#pragma unroll
      for (int hh = 0; hh < 4; ++hh) {
        *(LAS f32x4*)(rec + hh * 128 + 8 * l) = f32x4{acc[0][hh], acc[1][hh], acc[2][hh], acc[3][hh]};
        *(LAS f32x4*)(rec + hh * 128 + 8 * l + 4) = f32x4{acc[4][hh], acc[5][hh], acc[6][hh], acc[7][hh]};
      }
      if (l < 4) { ml[w * 8 + l * 2] = m; ml[w * 8 + l * 2 + 1] = lsum; }
    }
  } else {
    const int hw = w - 4;
    __builtin_amdgcn_s_setprio(3);
    u32x4 x0 = *(const GAS u32x4*)(x + l * 16), x1 = *(const GAS u32x4*)(x + 1024 + l * 16);
    u32x2 xw = *(const GAS u32x2*)(x + (l >> 5) * 1024 + (128 * hw + 4 * (l & 31)) * 2);
    cfence();
    u32x4 wqr[4];
#pragma unroll
    for (int i = 0; i < 4; ++i) {
      const int row = 128 * hw + 32 * i + (l >> 1);
      wqr[i] = ldnt((const GAS unsigned char*)wq_ + ((size_t)row * 4096 + kvh * 512 + j * 16 + (l & 1) * 8) * 2);
    }
    u64 E = ld_sc1((const GAS u64*)(ws + WS_EPOCH));
    cfence();
    u32x4 wa[8];
    {
      const __amdgpu_buffer_rsrc_t rw = __builtin_amdgcn_make_buffer_rsrc((void*)(hw == 3 ? w3_ : w1_), 0, DM * FF * 2, 0x00020000);
      const int wofs = (hw >= 2 && ffn) ? ((rb * 32 + 8 * g) * FF + cb * 128 + (l & 15) * 8) * 2 : OOB;
#pragma unroll
      for (int t = 0; t < 8; ++t) wa[t] = __builtin_amdgcn_raw_buffer_load_b128(rw, wofs, t * FF * 2, AUX_NT);
    }
    cfence();
    asm volatile("s_barrier" ::: "memory");
    STAMP(1);
    asm volatile("" : "+v"(x0), "+v"(x1), "+v"(xw));
    {
      float s0 = 0.f, s1 = 0.f;
#pragma unroll
      for (int i = 0; i < 4; ++i) { s0 += blo(x0[i]) + bhi(x0[i]); s1 += blo(x1[i]) + bhi(x1[i]); }
      const float mean0 = wave_sum(s0) * (1.0f / DM), mean1 = wave_sum(s1) * (1.0f / DM);
      float q0 = 0.f, q1 = 0.f;
#pragma unroll
      for (int i = 0; i < 4; ++i) {
        float d;
        d = blo(x0[i]) - mean0; q0 += d * d; d = bhi(x0[i]) - mean0; q0 += d * d;
        d = blo(x1[i]) - mean1; q1 += d * d; d = bhi(x1[i]) - mean1; q1 += d * d;
      }
      const float var0 = wave_sum(q0) * (1.0f / DM), var1 = wave_sum(q1) * (1.0f / DM);
      const float mb = rbf(l < 32 ? mean0 : mean1);
      const float den = rbf(sqrtf(rbf(rbf(l < 32 ? var0 : var1) + 1e-5f)));
      *(LAS u32x2*)(L + O_XN + (l >> 5) * 1024 + (128 * hw + 4 * (l & 31)) * 2) =
          u32x2{pack2(rbf(blo(xw[0]) - mb) / den, rbf(bhi(xw[0]) - mb) / den), pack2(rbf(blo(xw[1]) - mb) / den, rbf(bhi(xw[1]) - mb) / den)};
    }
    STAMP(2);
#pragma unroll
    for (int i = 0; i < 4; ++i) *(LAS u32x4*)(L + O_WQ + hw * 4096 + i * 1024 + l * 16) = wqr[i];
    lds_wait();
    STAMP(3);
    LAS float* part = (LAS float*)(L + O_PART);
    {
      f32x4 dq = {0, 0, 0, 0};
#pragma unroll
      for (int s = 0; s < 4; ++s) {
        const u32x4 af = *(LAS const u32x4*)(L + O_XN + (l & 1) * 1024 + (128 * hw + 32 * s + 8 * g) * 2);
        const int ro = (32 * s + 8 * g + qq) * 32 + p4 * 8;
        const u32x2 b0 = tr_read(L + O_WQ + hw * 4096 + ro), b1 = tr_read(L + O_WQ + hw * 4096 + ro + 128);
        dq = mfma16(af, u32x4{b0[0], b0[1], b1[0], b1[1]}, dq);
      }
      if (l < 16) { part[hw * 32 + l] = dq[0]; part[hw * 32 + 16 + l] = dq[1]; }
    }
    lds_arrive(cnt, l);
    asm volatile("" : "+v"(E));
    const u64 T = E + 1;
    STAMP(4);
    if (hw == 0) {
      lds_wait_cnt(cnt, 4);
      if (l < 32) {
        const float s = (part[l] + part[32 + l]) + (part[64 + l] + part[96 + l]);
        st_sc1((GAS u64*)(ws + WS_QBOX) + ((l >> 4) * 8 + kvh) * 512 + j * 16 + (l & 15), (T << 16) | (pack2(s, s) & 0xffffu));
      }
      cfence();
      lds_arrive(cnt + 2, l);
    } else if (hw == 1) {
      lds_wait_cnt(cnt + 4, 4);
      if (l == 0) st_sc1((GAS u64*)(ws + WS_FLAG) + bid, T);
    } else if (ffn) {
      lds_wait_cnt(cnt, 4);
      const u32x4 xf = *(LAS const u32x4*)(L + O_XN + (l & 1) * 1024 + (rb * 32 + 8 * g) * 2);
      f32x4 r[4];
#pragma unroll
      for (int e = 0; e < 8; ++e) {
        const int q2 = e >> 1;
        const unsigned sel = (e & 1) ? 0x07060302u : 0x05040100u;
        const u32x4 fa = {__builtin_amdgcn_perm(wa[1][q2], wa[0][q2], sel), __builtin_amdgcn_perm(wa[3][q2], wa[2][q2], sel),
                          __builtin_amdgcn_perm(wa[5][q2], wa[4][q2], sel), __builtin_amdgcn_perm(wa[7][q2], wa[6][q2], sel)};
        const f32x4 da = mfma16(xf, fa, f32x4{0, 0, 0, 0});
        r[e >> 2][e & 3] = da[0]; r[2 + (e >> 2)][e & 3] = da[1];
      }
      LAS unsigned char* gu = L + O_GU + (hw - 2) * 1280;
      if (l < 16) {
#pragma unroll
        for (int i = 0; i < 4; ++i) *(LAS f32x4*)(gu + l * 80 + i * 16) = r[i];
      }
      lds_wait();
      const f32x4 gv = *(LAS f32x4*)(gu + (l >> 2) * 80 + (l & 3) * 16);
      lds_wait_cnt(cnt + 4, 4);
      ST16(san4(gv), myin + IN_GU + (hw - 2) * 1024 + l * 16);
    }
    STAMP(5);
    while (__builtin_amdgcn_s_memrealtime() - t_entry < QPOLL_T) __builtin_amdgcn_s_sleep(2);
    {
      const int qoff = WS_QBOX + (gid * 512 + (tid - 256) * 2) * 8;
      const unsigned thi = (unsigned)(T >> 16), tlo = (unsigned)T & 0xffffu;
      u32x4 qg = LD16(qoff);
      cfence();
      lds_arrive(cnt + 3, l);
      asm volatile("" : "+v"(qg));
      unsigned spins = 0;
      while (qg[1] != thi || qg[3] != thi || (qg[0] >> 16) != tlo || (qg[2] >> 16) != tlo) {
        if (++spins > SPIN_CAP) break;
        DIAG_RETRY;
        asm volatile("" ::: "memory");
        qg = LD16(qoff);
      }
      *(LAS unsigned*)(L + O_Q + (tid - 256) * 4) = (qg[0] & 0xffffu) | (qg[2] << 16);
      lds_arrive(cnt + 1, l);
    }
    STAMP(6);
    if (hw == 0) {
      u32x4 w2r[7];
#pragma unroll
      for (int i = 0; i < 7; ++i) w2r[i] = ldnt((const GAS unsigned char*)w2_ + ((size_t)(56 * j + 8 * i + (l >> 3)) * 512 + 64 * kvh + (l & 7) * 8) * 2);
      cfence();
      {
        const unsigned tl = (unsigned)T, th = (unsigned)(T >> 32);
        unsigned spins = 0;
        for (;;) {
          asm volatile("" ::: "memory");
          const u32x4 f0 = LD16(WS_FLAG + l * 32), f1 = LD16(WS_FLAG + l * 32 + 16);
          const bool ok = f0[0] == tl && f0[1] == th && f0[2] == tl && f0[3] == th && f1[0] == tl && f1[1] == th && f1[2] == tl && f1[3] == th;
          if (__all(ok) || ++spins > SPIN_CAP) break;
        }
        cfence();
      }
      Tep = T;
      STAMP(7);
#pragma unroll
      for (int i = 0; i < 7; ++i) *(LAS u32x4*)(L + O_W2 + (8 * i + (l >> 3)) * W2_STR + (l & 7) * 16) = w2r[i];
      if (ffn) {
        const int po = WS_IN + ffn_wg(cb * 16 + (l >> 2)) * IN_STR + IN_GU + rb * 64 + (l & 3) * 16;
        u32x4 g0, g1;
        unsigned spins = 0;
        for (;;) {
          asm volatile("" ::: "memory");
          g0 = LD16(po); g1 = LD16(po + 1024);
          if (__all(ok4(g0) && ok4(g1)) || ++spins > SPIN_CAP) break;
          __builtin_amdgcn_s_sleep(8);
        }
        STAMP(13);
        float hv[4];
#pragma unroll
        for (int n = 0; n < 4; ++n) {
          float tg = asf(g0[n]), tu = asf(g1[n]);
          tg += dppf<ROR4>(tg); tg += dppf<ROR8>(tg); tg = rbf(xrow_sum(tg));
          tu += dppf<ROR4>(tu); tu += dppf<ROR8>(tu); tu = rbf(xrow_sum(tu));
          hv[n] = rbf(silu(tg)) * tu;
        }
        u32x4 hp;
#pragma unroll
        for (int n = 0; n < 4; ++n) { const unsigned u = pack2(hv[n], dppf<XOR2>(hv[n])); hp[n] = u == INVAL ? 0x7FC07FC0u : u; }
        if (l < 2) ST16(hp, WS_HB + fs * 32 + l * 16);
      }
      {
        u32x4 hg;
        unsigned spins = 0;
        for (;;) {
          asm volatile("" ::: "memory");
          hg = LD16(l < 14 ? WS_HB + j * 224 + l * 16 : OOB);
          if (__all(ok4(hg)) || ++spins > SPIN_CAP) break;
          __builtin_amdgcn_s_sleep(8);
        }
        if (l < 16) {
          *(LAS u32x2*)(L + O_H + l * 8) = u32x2{__builtin_amdgcn_perm(hg[1], hg[0], 0x05040100u), __builtin_amdgcn_perm(hg[3], hg[2], 0x05040100u)};
          *(LAS u32x2*)(L + O_H + 128 + l * 8) = u32x2{__builtin_amdgcn_perm(hg[1], hg[0], 0x07060302u), __builtin_amdgcn_perm(hg[3], hg[2], 0x07060302u)};
        }
        lds_wait();
        f32x4 o0, o1;
#pragma unroll
        for (int n = 0; n < 4; ++n) {
          f32x4 d = {0, 0, 0, 0};
#pragma unroll
          for (int ks = 0; ks < 2; ++ks) {
            const u32x4 af = *(LAS const u32x4*)(L + O_H + (l & 1) * 128 + (32 * ks + 8 * g) * 2);
            const int r0 = min(32 * ks + 8 * g + qq, 51);
            const int ro = r0 * W2_STR + (16 * n + 4 * p4) * 2;
            const u32x2 b0 = tr_read(L + O_W2 + ro), b1 = tr_read(L + O_W2 + ro + 4 * W2_STR);
            d = mfma16(af, u32x4{b0[0], b0[1], b1[0], b1[1]}, d);
          }
          o0[n] = d[0]; o1[n] = d[1];
        }
        if (l < 16) { *(LAS f32x4*)(L + O_F + l * 16) = o0; *(LAS f32x4*)(L + O_F + 256 + l * 16) = o1; }
      }
      STAMP(8);
    } else if (hw == 1) {
      const GAS unsigned char* wb = (const GAS unsigned char*)wo_ + ((size_t)(akvh * 512 + 32 * asp) * 512 + l * 8) * 2;
#pragma unroll 1
      for (int h = 0; h < 2; ++h) {
        u32x4 wr[16];
#pragma unroll
        for (int i = 0; i < 16; ++i) wr[i] = ldw<NT_WO>(wb + (size_t)(h * 16 + i) * 1024);
        cfence();
#pragma unroll
        for (int i = 0; i < 16; ++i) *(LAS u32x4*)(L + O_WO + (h * 16 + i) * WO_STR + l * 16) = wr[i];
      }
      STAMP(8);
    } else if (hw == 2) {
      while (__builtin_amdgcn_s_memrealtime() - t_entry < T_C) __builtin_amdgcn_s_sleep(16);
      const unsigned prog = __hip_atomic_load(cnt + 6, __ATOMIC_RELAXED, __HIP_MEMORY_SCOPE_WORKGROUP);
      if (l == 0) st_sc1((GAS u64*)(ws + WS_PROG) + bid, (T << 16) | (prog & 0xffffu));
      const u64 want = T & 0xFFFFFFFFFFFFull;
      u64 pg;
      unsigned spins = 0;
      for (;;) {
        pg = ld_sc1((const GAS u64*)(ws + WS_PROG) + gid * 16 + (l & 15));
        if (__all((pg >> 16) == want) || ++spins > SPIN_CAP) break;
        __builtin_amdgcn_s_sleep(8);
      }
      STAMP(7);
#define KAPPA 0.9f
      float pr = (float)(unsigned)(pg & 0xffffu);
      pr = 0.5f * (pr + dppf<ROR8>(pr));
      float sp_ = pr;
      sp_ += dppf<XOR1>(sp_); sp_ += dppf<XOR2>(sp_); sp_ += dppf<ROR4>(sp_); sp_ += dppf<ROR8>(sp_);
      float n = fmaxf((float)PPM + KAPPA * (pr - sp_ * (1.0f / 16.0f)), 0.0f);
      float tot = n;
      tot += dppf<XOR1>(tot); tot += dppf<XOR2>(tot); tot += dppf<ROR4>(tot); tot += dppf<ROR8>(tot);
      n *= (16.0f * PPM) / fmaxf(tot, 1e-3f);
      int lo = 0, hi = 0;
      float cum = 0.f;
#pragma unroll
      for (int i = 0; i < 16; ++i) {
        const float ni = __builtin_bit_cast(float, __builtin_amdgcn_readlane(asu(n), i));
        const int before = min((int)(cum + 0.5f), 16 * PPM);
        cum += ni;
        const int after = i == 15 ? 16 * PPM : min((int)(cum + 0.5f), 16 * PPM);
        if (i == asp) { lo = before; hi = after; }
      }
#define PLAN 1
      if (spins > SPIN_CAP || !PLAN) { lo = asp * PPM; hi = lo + PPM; }
      if (l == 0) { cnt[8] = lo; cnt[9] = hi - lo; }
      lds_arrive(cnt + 7, l);
      STAMP(8);
    }
    __builtin_amdgcn_s_setprio(0);
  }
  const float xv = (float)((const GAS __bf16*)x_)[b * 512 + 64 * (cw >> 4) + 16 * (l & 3) + (cw & 15)];
  wg_barrier();
  STAMP(9);
  if (w < 2) {
    LAS float* ml = (LAS float*)(L + O_MLW);
    const int hh = tid >> 5;
    float M = -INFINITY;
#pragma unroll
    for (int ww = 0; ww < 4; ++ww) M = fmaxf(M, ml[ww * 8 + hh * 2]);
    f32x4 o = {0, 0, 0, 0};
    float Ls = 0.f;
#pragma unroll
    for (int ww = 0; ww < 4; ++ww) {
      const float f = __builtin_amdgcn_exp2f((ml[ww * 8 + hh * 2] - M) * LOG2E);
      o += f * *(LAS f32x4*)(L + O_REC + ww * 2048 + tid * 16);
      Ls += f * ml[ww * 8 + hh * 2 + 1];
    }
    const int cin = WS_IN + (gid * 16 + (tid >> 3)) * IN_STR;
    ST16(san4(o), cin + IN_H1 + asp * 128 + (tid & 7) * 16);
    if ((tid & 7) == 0) ST8((u32x2{san(M), san(Ls)}), cin + IN_ML + asp * 8);
  } else if (w == 2) {
    if (l < 32) ST16(san4(*(LAS f32x4*)(L + O_F + l * 16)), myin + IN_F2 + l * 16);
  } else if (bid == 0 && tid == 256) {
    st_sc1((GAS u64*)(ws + WS_EPOCH), Tep);
  }
  STAMP(10);
  u32x4 wf[4];
#pragma unroll
  for (int n = 0; n < 4; ++n) {
    const int ro = (8 * g + qq) * WO_STR + (64 * w + 16 * n + 4 * p4) * 2;
    const u32x2 b0 = tr_read(L + O_WO + ro), b1 = tr_read(L + O_WO + ro + 4 * WO_STR);
    wf[n] = u32x4{b0[0], b0[1], b1[0], b1[1]};
  }
  if (w == 0) {
    u32x4 o0 = LD16(myin + IN_H1 + l * 16), o1 = LD16(myin + IN_H1 + 1024 + l * 16);
    u32x2 mlg = LD8(myin + IN_ML + (l & 15) * 8);
    unsigned spins = 0;
    while (!__all(ok4(o0) && ok4(o1) && mlg[0] != INVAL && mlg[1] != INVAL) && ++spins <= SPIN_CAP) {
      __builtin_amdgcn_s_sleep(TSLEEP);
      asm volatile("" ::: "memory");
      const bool k0 = ok4(o0), k1 = ok4(o1), k2 = mlg[0] != INVAL && mlg[1] != INVAL;
      const u32x4 n0 = LD16(k0 ? OOB : myin + IN_H1 + l * 16), n1 = LD16(k1 ? OOB : myin + IN_H1 + 1024 + l * 16);
      const u32x2 n2 = LD8(k2 ? OOB : myin + IN_ML + (l & 15) * 8);
      o0 = k0 ? o0 : n0; o1 = k1 ? o1 : n1; mlg = k2 ? mlg : n2;
    }
    STAMP(11);
    const float mp = asf(mlg[0]), lp = asf(mlg[1]);
    float M = mp;
    M = fmaxf(M, dppf<XOR1>(M)); M = fmaxf(M, dppf<XOR2>(M)); M = fmaxf(M, dppf<ROR4>(M)); M = fmaxf(M, dppf<ROR8>(M));
    const float f = __builtin_amdgcn_exp2f((mp - M) * LOG2E);
    float Ls = f * lp;
    Ls += dppf<XOR1>(Ls); Ls += dppf<XOR2>(Ls); Ls += dppf<ROR4>(Ls); Ls += dppf<ROR8>(Ls);
    const float f0 = __shfl(f, l >> 3), f1 = __shfl(f, 8 + (l >> 3));
    float ov[4];
#pragma unroll
    for (int n = 0; n < 4; ++n) { float t = asf(o0[n]) * f0 + asf(o1[n]) * f1; t += dppf<ROR8>(t); ov[n] = xrow_sum(t) / Ls; }
    if (l < 8) *(LAS u32x2*)(L + O_O + l * 8) = u32x2{pack2(ov[0], ov[1]), pack2(ov[2], ov[3])};
  }
  wg_barrier();
  {
    const u32x4 af = *(LAS const u32x4*)(L + O_O + g * 16);
    f32x4 so;
#pragma unroll
    for (int n = 0; n < 4; ++n) so[n] = mfma16(af, wf[n], f32x4{0, 0, 0, 0})[0];
    if (l < 16) ST16(san4(so), myin + IN_H2 + (16 * w + l) * 16);
  }
  if (w != 6) STAMP(12);
  if (w == 1) {
    const int fo = l < 32 ? WS_IN + ((l << 3) | kvh) * IN_STR + IN_F2 + (b * 16 + sp) * 16 : OOB;
    u32x4 fp = LD16(fo);
    unsigned spins = 0;
    while (!__all(ok4(fp)) && ++spins <= SPIN_CAP) {
      __builtin_amdgcn_s_sleep(2 * TSLEEP);
      asm volatile("" ::: "memory");
      const bool k0 = ok4(fp);
      const u32x4 n0 = LD16(k0 ? OOB : fo);
      fp = k0 ? fp : n0;
    }
    float f[4];
#pragma unroll
    for (int n = 0; n < 4; ++n) f[n] = wave_sum(asf(fp[n]));
    const float fv = l == 0 ? f[0] : l == 1 ? f[1] : l == 2 ? f[2] : f[3];
    if (l < 4) ((LAS float*)(L + O_FG))[l] = rbf(silu(rbf(fv)));
    lds_arrive(cnt + 5, l);
    STAMP(13);
  }
  if (w == 0) {
    const int so0 = WS_IN + (b * 128 + l) * IN_STR + IN_H2 + cw * 16, so1 = so0 + 64 * IN_STR;
    u32x4 s0 = LD16(so0), s1 = LD16(so1);
    unsigned spins = 0;
    while (!__all(ok4(s0) && ok4(s1)) && ++spins <= SPIN_CAP) {
      __builtin_amdgcn_s_sleep(TSLEEP);
      asm volatile("" ::: "memory");
      const bool k0 = ok4(s0), k1 = ok4(s1);
      const u32x4 n0 = LD16(k0 ? OOB : so0), n1 = LD16(k1 ? OOB : so1);
      s0 = k0 ? s0 : n0; s1 = k1 ? s1 : n1;
    }
    STAMP(13);
    float f[4];
#pragma unroll
    for (int n = 0; n < 4; ++n) f[n] = wave_sum(asf(s0[n]) + asf(s1[n]));
    const float s = l == 0 ? f[0] : l == 1 ? f[1] : l == 2 ? f[2] : f[3];
    lds_wait_cnt(cnt + 5, 1);
    if (l < 4) ((GAS __bf16*)out_)[b * 512 + 64 * (cw >> 4) + 16 * l + (cw & 15)] = (__bf16)(xv + rbf(rbf(s) * ((LAS float*)(L + O_FG))[l]));
  }
  STAMP(14);
}

extern "C" void kernel_launch(void* const* d_in, const int* in_sizes, int n_in, void* d_out, int out_size, void* d_ws, size_t ws_size,
                              hipStream_t stream) {
  k_fused<<<NWG, NTHR, 0, stream>>>((const __hip_bfloat16*)d_in[0], (const __hip_bfloat16*)d_in[1], (const __hip_bfloat16*)d_in[2],
                                    (const __hip_bfloat16*)d_in[3], (const __hip_bfloat16*)d_in[4], (const __hip_bfloat16*)d_in[6],
                                    (const __hip_bfloat16*)d_in[7], (const __hip_bfloat16*)d_in[8], (unsigned char*)d_ws,
                                    (__hip_bfloat16*)d_out);
}
```

```cpp
#include <hip/hip_runtime.h>
#include <hip/hip_bf16.h>
#include <stdint.h>

#define GAS __attribute__((address_space(1)))
#define LAS __attribute__((address_space(3)))
typedef unsigned u32x4 __attribute__((ext_vector_type(4)));
typedef unsigned u32x2 __attribute__((ext_vector_type(2)));
typedef float f32x4 __attribute__((ext_vector_type(4)));
typedef short s16x8 __attribute__((ext_vector_type(8)));
typedef short s16x4 __attribute__((ext_vector_type(4)));
typedef __bf16 bf16x2_t __attribute__((ext_vector_type(2)));
typedef unsigned long long u64;
typedef GAS u64 gu64;

constexpr int DM = 512, SK = 32768, FF = 1792;
constexpr int NWG = 256, NTHR = 512, NSPLIT = 16;
constexpr int KEYS_WG = SK / NSPLIT;
constexpr int NTILE = KEYS_WG / (32 * 4);
constexpr float SCALE = 0.08838834764831845f;
constexpr float LOG2E = 1.4426950408889634f;
constexpr unsigned SPIN_CAP = 1u << 15;
constexpr unsigned INVAL = 0xFFFFFFFFu;
constexpr unsigned QPOLL_T = 460;
constexpr int PSTAG = 8;
constexpr int SHIFT = 2;
constexpr int OOB = 0x7ff00000;

constexpr int WS_EPOCH = 0;
constexpr int WS_FLAG = 2048;
constexpr int WS_QBOX = 8192;
constexpr int WS_HB = WS_QBOX + 65536;
constexpr int WS_IN = WS_HB + 8192;
constexpr int IN_H1 = 0;
constexpr int IN_H2 = 2048;
constexpr int IN_GU = 4096;
constexpr int IN_F2 = 6144;
constexpr int IN_ML = 6656;
constexpr int IN_STR = 7168;
constexpr int WS_END = WS_IN + NWG * IN_STR;

constexpr int WO_STR = 1056;
constexpr int O_XN = 0;
constexpr int O_WQ = 2048;
constexpr int W2_STR = 160;
constexpr int O_W2 = O_WQ + 16384;
constexpr int O_GU = O_W2 + 56 * W2_STR + 64;
constexpr int O_PART = O_GU + 2 * 16 * 80;
constexpr int O_H = O_PART + 512;
constexpr int O_Q = O_H + 256;
constexpr int O_WO = O_Q + 1024;
constexpr int O_REC = O_WO + 32 * WO_STR;
constexpr int O_F = O_REC + 8192;
constexpr int O_MLW = O_F + 512;
constexpr int O_O = O_MLW + 128;
constexpr int O_FG = O_O + 64;
constexpr int O_CNT = O_FG + 16;
constexpr int LDS_BYTES = 84 * 1024;
static_assert(O_CNT + 48 <= LDS_BYTES, "");

#define RLX_AGENT __ATOMIC_RELAXED, __HIP_MEMORY_SCOPE_AGENT
__device__ __forceinline__ float asf(unsigned u) { return __builtin_bit_cast(float, u); }
__device__ __forceinline__ unsigned asu(float f) { return __builtin_bit_cast(unsigned, f); }
__device__ __forceinline__ unsigned san(float f) { const unsigned u = asu(f); return u == INVAL ? 0x7FC00000u : u; }
__device__ __forceinline__ u32x4 san4(f32x4 v) { return u32x4{san(v[0]), san(v[1]), san(v[2]), san(v[3])}; }
__device__ __forceinline__ bool ok4(u32x4 v) { return v[0] != INVAL && v[1] != INVAL && v[2] != INVAL && v[3] != INVAL; }
__device__ __forceinline__ unsigned pack2(float lo, float hi) {
  bf16x2_t v = {(__bf16)lo, (__bf16)hi};
  return __builtin_bit_cast(unsigned, v);
}
__device__ __forceinline__ float rbf(float x) { return (float)(__bf16)x; }
__device__ __forceinline__ float blo(unsigned u) { return asf(u << 16); }
__device__ __forceinline__ float bhi(unsigned u) { return asf(u & 0xffff0000u); }
__device__ __forceinline__ f32x4 mfma16(u32x4 a, u32x4 b, f32x4 c) {
  return __builtin_amdgcn_mfma_f32_16x16x32_bf16(__builtin_bit_cast(s16x8, a), __builtin_bit_cast(s16x8, b), c, 0, 0, 0);
}
template <int CTRL> __device__ __forceinline__ float dppf(float x) {
  return asf(__builtin_amdgcn_mov_dpp(asu(x), CTRL, 0xf, 0xf, true));
}
template <int CTRL> __device__ __forceinline__ unsigned dppu(unsigned x) {
  return __builtin_amdgcn_mov_dpp(x, CTRL, 0xf, 0xf, true);
}
template <int CTRL, int BANK> __device__ __forceinline__ float updf(float old, float x) {
  return asf(__builtin_amdgcn_update_dpp(asu(old), asu(x), CTRL, 0xf, BANK, false));
}
constexpr int ROR4 = 0x124, ROR8 = 0x128, ROR12 = 0x12C, QID = 0xE4, XOR1 = 0xB1, XOR2 = 0x4E;
__device__ __forceinline__ float xrow_max(float x) {
  auto s = __builtin_amdgcn_permlane16_swap(asu(x), asu(x), false, false);
  x = fmaxf(asf(s[0]), asf(s[1]));
  auto t = __builtin_amdgcn_permlane32_swap(asu(x), asu(x), false, false);
  return fmaxf(asf(t[0]), asf(t[1]));
}
__device__ __forceinline__ float xrow_sum(float x) {
  auto s = __builtin_amdgcn_permlane16_swap(asu(x), asu(x), false, false);
  x = asf(s[0]) + asf(s[1]);
  auto t = __builtin_amdgcn_permlane32_swap(asu(x), asu(x), false, false);
  return asf(t[0]) + asf(t[1]);
}
__device__ __forceinline__ float wave_sum(float x) {
  x += dppf<XOR1>(x); x += dppf<XOR2>(x); x += dppf<ROR4>(x); x += dppf<ROR8>(x);
  return xrow_sum(x);
}
__device__ __forceinline__ u32x2 tr_read(LAS const unsigned char* p) {
  return __builtin_bit_cast(u32x2, __builtin_amdgcn_ds_read_tr16_b64_v4i16((LAS s16x4*)p));
}
__device__ __forceinline__ float silu(float x) { return x / (1.0f + __expf(-x)); }
__device__ __forceinline__ void wg_barrier() { asm volatile("s_waitcnt lgkmcnt(0)\n\ts_barrier" ::: "memory"); }
__device__ __forceinline__ void lds_wait() { asm volatile("s_waitcnt lgkmcnt(0)" ::: "memory"); }
__device__ __forceinline__ void cfence() { asm volatile("" ::: "memory"); __builtin_amdgcn_sched_barrier(0); }
__device__ __forceinline__ u32x4 ldnt(const GAS unsigned char* p) { return __builtin_nontemporal_load((const GAS u32x4*)p); }
__device__ __forceinline__ u64 ld_sc1(const GAS u64* p) { return __hip_atomic_load((gu64*)p, RLX_AGENT); }
__device__ __forceinline__ void st_sc1(GAS u64* p, u64 v) { __hip_atomic_store(p, v, RLX_AGENT); }
constexpr int AUX_SC1 = 16, AUX_NT = 2;
#define LD16(off) __builtin_amdgcn_raw_buffer_load_b128(rs, (off), 0, AUX_SC1)
#define LD8(off) __builtin_amdgcn_raw_buffer_load_b64(rs, (off), 0, AUX_SC1)
#define ST16(v, off) __builtin_amdgcn_raw_buffer_store_b128((v), rs, (off), 0, AUX_SC1)
#define ST8(v, off) __builtin_amdgcn_raw_buffer_store_b64((v), rs, (off), 0, AUX_SC1)
__device__ __forceinline__ void lds_arrive(LAS unsigned* c, int l) {
  asm volatile("s_waitcnt lgkmcnt(0)" ::: "memory");
  if (l == 0) __hip_atomic_fetch_add(c, 1u, __ATOMIC_RELAXED, __HIP_MEMORY_SCOPE_WORKGROUP);
}
__device__ __forceinline__ void lds_wait_cnt(LAS unsigned* c, unsigned n) {
  while (__builtin_amdgcn_readfirstlane(__hip_atomic_load(c, __ATOMIC_RELAXED, __HIP_MEMORY_SCOPE_WORKGROUP)) < n) __builtin_amdgcn_s_sleep(1);
  asm volatile("" ::: "memory");
}
__device__ __forceinline__ int ffn_wg(int fs) { return ((fs % 28) << 3) | (fs / 28); }

__device__ __forceinline__ float qk_reduce(const f32x4 d0, const f32x4 d1, const f32x4 d2, const f32x4 d3) {
  float z0 = d0[0];
  z0 = updf<QID, 0x2>(z0, d1[1]); z0 = updf<QID, 0x4>(z0, d2[2]); z0 = updf<QID, 0x8>(z0, d3[3]);
  float z1 = dppf<ROR4>(d0[3]);
  z1 = updf<ROR4, 0x2>(z1, d1[0]); z1 = updf<ROR4, 0x4>(z1, d2[1]); z1 = updf<ROR4, 0x8>(z1, d3[2]);
  float z2 = dppf<ROR8>(d0[2]);
  z2 = updf<ROR8, 0x2>(z2, d1[3]); z2 = updf<ROR8, 0x4>(z2, d2[0]); z2 = updf<ROR8, 0x8>(z2, d3[1]);
  float z3 = dppf<ROR12>(d0[1]);
  z3 = updf<ROR12, 0x2>(z3, d1[2]); z3 = updf<ROR12, 0x4>(z3, d2[3]); z3 = updf<ROR12, 0x8>(z3, d3[0]);
  return (z0 + z1) + (z2 + z3);
}


__global__ void __launch_bounds__(NTHR, 2)
k_fused(const __hip_bfloat16* __restrict__ x_, const __hip_bfloat16* __restrict__ k_, const __hip_bfloat16* __restrict__ v_,
        const __hip_bfloat16* __restrict__ wq_, const __hip_bfloat16* __restrict__ wo_, const __hip_bfloat16* __restrict__ w1_,
        const __hip_bfloat16* __restrict__ w3_, const __hip_bfloat16* __restrict__ w2_, unsigned char* ws_,
        __hip_bfloat16* __restrict__ out_) {
  __shared__ __attribute__((aligned(16))) unsigned char lds[LDS_BYTES];
  LAS unsigned char* L = (LAS unsigned char*)lds;
  LAS unsigned* cnt = (LAS unsigned*)(L + O_CNT);
  GAS unsigned char* ws = (GAS unsigned char*)ws_;
  const u64 t_entry = __builtin_amdgcn_s_memrealtime();
  const int bid = blockIdx.x, kvh = bid & 7, j = bid >> 3, b = j >> 4, sp = j & 15;
  const int gid = bid >> 4, akvh = gid & 7, asp = bid & 15;
  const int tid = threadIdx.x, l = tid & 63, w = __builtin_amdgcn_readfirstlane(tid >> 6);
  const int g = l >> 4, qq = (l >> 2) & 3, p4 = l & 3;
  const int cw = kvh * 16 + sp;
  const int myin = WS_IN + bid * IN_STR;
  const bool ffn = j < 28;
  const int fs = kvh * 28 + (ffn ? j : 0);
  const int rb = fs & 15, cb = fs >> 4;
  const __amdgpu_buffer_rsrc_t rs = __builtin_amdgcn_make_buffer_rsrc((void*)ws_, 0, WS_END, 0x00020000);
  const GAS unsigned char* x = (const GAS unsigned char*)x_;
  float xv = 0.f;
  if (w < 4) {
    const size_t kvbase = (size_t)gid * SK * 256;
    const __amdgpu_buffer_rsrc_t rk = __builtin_amdgcn_make_buffer_rsrc((void*)((const unsigned char*)k_ + kvbase), 0, SK * 256, 0x00020000);
    const __amdgpu_buffer_rsrc_t rv = __builtin_amdgcn_make_buffer_rsrc((void*)((const unsigned char*)v_ + kvbase), 0, SK * 256, 0x00020000);
    const int so0 = (asp * 64 + w) * 8192;
    const int koff = (qq * 16 + g * 4 + p4) * 16, voff = l * 16;
    u32x4 K[2][8], V[2][8];
#define LOADK(c_, so_) _Pragma("unroll") for (int t = 0; t < 8; ++t) K[c_][t] = __builtin_amdgcn_raw_buffer_load_b128(rk, koff + t * 1024, (so_), AUX_NT)
#define LOADV(c_, so_) _Pragma("unroll") for (int t = 0; t < 8; ++t) V[c_][t] = __builtin_amdgcn_raw_buffer_load_b128(rv, voff + t * 1024, (so_), AUX_NT)
    ST16((u32x4{INVAL, INVAL, INVAL, INVAL}), myin + w * 2048 + l * 16);
    ST16((u32x4{INVAL, INVAL, INVAL, INVAL}), w < 3 ? myin + w * 2048 + 1024 + l * 16 : (ffn && l < 2) ? WS_HB + fs * 32 + l * 16 : OOB);
    if (tid < 3) *(LAS u32x4*)(cnt + 4 * tid) = u32x4{0u, 0u, 0u, 0u};
    cfence();
    LOADK(0, so0);
    cfence();
    wg_barrier();
    asm volatile("s_waitcnt vmcnt(8)" ::: "memory");
    lds_arrive(cnt + 4, l);
    lds_wait_cnt(cnt + 2, 1);
    LOADV(0, so0);
    LOADK(1, so0 + 4 * 8192);
    cfence();
    lds_wait_cnt(cnt + 3, 4);
    LOADV(1, so0 + 4 * 8192);
    cfence();
    lds_wait_cnt(cnt + 1, 4);
    const u32x4 qf = *(LAS const u32x4*)(L + O_Q + (p4 * 128 + (4 * g + qq) * 8) * 2);
    f32x4 acc[8];
#pragma unroll
    for (int e = 0; e < 8; ++e) acc[e] = f32x4{0, 0, 0, 0};
    float m = -INFINITY, lsum = 0.f;
    int nx = 2;
    const int role = (bid & 1) ? 1 : 2;
    bool valid[2] = {true, true};
#pragma unroll 1
    do {
#pragma unroll
      for (int c = 0; c < 2; ++c) {
        f32x4 d[8];
#pragma unroll
        for (int t = 0; t < 8; ++t) d[t] = mfma16(K[c][t], qf, f32x4{0, 0, 0, 0});
        cfence();
        const bool cur = valid[c];
        int so = so0 + nx * (4 * 8192);
        if (nx >= NTILE - 1) {
          valid[c] = nx == NTILE - 1 ? !(role == 1 && w >= 4 - SHIFT) : (nx == NTILE && role == 2 && w < SHIFT);
          if (nx == NTILE) so = ((asp ^ 1) * 64 + 64 - SHIFT + w) * 8192;
          if (!valid[c]) so = OOB;
        }
        LOADK(c, so);
        cfence();
        float S0 = qk_reduce(d[0], d[1], d[2], d[3]), S1 = qk_reduce(d[4], d[5], d[6], d[7]);
        if (!cur) S0 = S1 = -INFINITY;
        unsigned pk = pack2(S0, S1);
        pk = pack2(blo(pk) * SCALE, bhi(pk) * SCALE);
        const float a0 = blo(pk), a1 = bhi(pk);
        float tm = fmaxf(a0, a1);
        tm = fmaxf(tm, dppf<ROR4>(tm)); tm = fmaxf(tm, dppf<ROR8>(tm)); tm = xrow_max(tm);
        if (__any(tm > m)) {
          const float mn = fmaxf(m, tm);
          const float al = __builtin_amdgcn_exp2f((m - mn) * LOG2E);
          m = mn; lsum *= al;
          const float al0 = dppf<0x00>(al), al1 = dppf<0x55>(al), al2 = dppf<0xAA>(al), al3 = dppf<0xFF>(al);
#pragma unroll
          for (int e = 0; e < 8; ++e) { acc[e][0] *= al0; acc[e][1] *= al1; acc[e][2] *= al2; acc[e][3] *= al3; }
        }
        const float p0 = __builtin_amdgcn_exp2f((a0 - m) * LOG2E), p1 = __builtin_amdgcn_exp2f((a1 - m) * LOG2E);
        lsum += p0 + p1;
        const unsigned pw = pack2(p0, p1);
        const u32x4 pf = {pw, dppu<ROR12>(pw), dppu<ROR8>(pw), dppu<ROR4>(pw)};
#pragma unroll
        for (int e = 0; e < 8; ++e) {
          const int r = e >> 1;
          const unsigned sel = (e & 1) ? 0x07060302u : 0x05040100u;
          const u32x4 bf = {__builtin_amdgcn_perm(V[c][4][r], V[c][0][r], sel), __builtin_amdgcn_perm(V[c][5][r], V[c][1][r], sel),
                            __builtin_amdgcn_perm(V[c][6][r], V[c][2][r], sel), __builtin_amdgcn_perm(V[c][7][r], V[c][3][r], sel)};
          acc[e] = mfma16(pf, bf, acc[e]);
        }
        cfence();
        LOADV(c, so);
        cfence();
        ++nx;
      }
    } while (valid[0] || valid[1]);
    lsum += dppf<ROR4>(lsum); lsum += dppf<ROR8>(lsum); lsum = xrow_sum(lsum);
    LAS float* rec = (LAS float*)(L + O_REC) + w * 512;
    LAS float* ml = (LAS float*)(L + O_MLW);
    if (l < 16) {
#pragma unroll
      for (int hh = 0; hh < 4; ++hh) {
        *(LAS f32x4*)(rec + hh * 128 + 8 * l) = f32x4{acc[0][hh], acc[1][hh], acc[2][hh], acc[3][hh]};
        *(LAS f32x4*)(rec + hh * 128 + 8 * l + 4) = f32x4{acc[4][hh], acc[5][hh], acc[6][hh], acc[7][hh]};
      }
      if (l < 4) { ml[w * 8 + l * 2] = m; ml[w * 8 + l * 2 + 1] = lsum; }
    }
    lds_wait();
    unsigned arr = 0;
    if (l == 0) arr = __hip_atomic_fetch_add(cnt + 6, 1u, __ATOMIC_RELAXED, __HIP_MEMORY_SCOPE_WORKGROUP);
    if (__builtin_amdgcn_readfirstlane(arr) == 3) {
      lds_wait_cnt(cnt + 7, 1);
#pragma unroll
      for (int i = 0; i < 2; ++i) {
        const int hh = 2 * i + (l >> 5);
        float M = -INFINITY;
#pragma unroll
        for (int ww = 0; ww < 4; ++ww) M = fmaxf(M, ml[ww * 8 + hh * 2]);
        f32x4 o = {0, 0, 0, 0};
        float Ls = 0.f;
#pragma unroll
        for (int ww = 0; ww < 4; ++ww) {
          const float f = __builtin_amdgcn_exp2f((ml[ww * 8 + hh * 2] - M) * LOG2E);
          o += f * *(LAS f32x4*)(L + O_REC + ww * 2048 + i * 1024 + l * 16);
          Ls += f * ml[ww * 8 + hh * 2 + 1];
        }
        const int cin = WS_IN + (gid * 16 + 8 * i + (l >> 3)) * IN_STR;
        ST16(san4(o), cin + IN_H1 + asp * 128 + (l & 7) * 16);
        ST8((u32x2{san(M), san(Ls)}), (l & 7) == 0 ? cin + IN_ML + asp * 8 : OOB);
      }
      const int a0 = myin + IN_H1 + l * 16, a1 = a0 + 1024, a2 = myin + IN_ML + (l & 15) * 8;
      u32x4 o0 = {INVAL, INVAL, INVAL, INVAL}, o1 = o0;
      u32x2 mlg = {INVAL, INVAL};
      u32x4 pa0 = LD16(a0), pa1 = LD16(a1);
      u32x2 pa2 = LD8(a2);
      __builtin_amdgcn_s_sleep(PSTAG);
      for (unsigned spins = 0; spins <= SPIN_CAP; ++spins) {
        asm volatile("" ::: "memory");
        bool k0 = ok4(o0), k1 = ok4(o1), k2 = mlg[0] != INVAL && mlg[1] != INVAL;
        const u32x4 pb0 = LD16(k0 ? OOB : a0), pb1 = LD16(k1 ? OOB : a1);
        const u32x2 pb2 = LD8(k2 ? OOB : a2);
        cfence();
        o0 = k0 ? o0 : pa0; o1 = k1 ? o1 : pa1; mlg = k2 ? mlg : pa2;
        const bool j0 = ok4(o0), j1 = ok4(o1), j2 = mlg[0] != INVAL && mlg[1] != INVAL;
        if (__all(j0 && j1 && j2)) break;
        asm volatile("" ::: "memory");
        pa0 = LD16(j0 ? OOB : a0); pa1 = LD16(j1 ? OOB : a1); pa2 = LD8(j2 ? OOB : a2);
        cfence();
        o0 = j0 ? o0 : pb0; o1 = j1 ? o1 : pb1; mlg = j2 ? mlg : pb2;
        if (__all(ok4(o0) && ok4(o1) && mlg[0] != INVAL && mlg[1] != INVAL)) break;
      }
      const float mp = asf(mlg[0]), lp = asf(mlg[1]);
      float M = mp;
      M = fmaxf(M, dppf<XOR1>(M)); M = fmaxf(M, dppf<XOR2>(M)); M = fmaxf(M, dppf<ROR4>(M)); M = fmaxf(M, dppf<ROR8>(M));
      const float f = __builtin_amdgcn_exp2f((mp - M) * LOG2E);
      float Ls = f * lp;
      Ls += dppf<XOR1>(Ls); Ls += dppf<XOR2>(Ls); Ls += dppf<ROR4>(Ls); Ls += dppf<ROR8>(Ls);
      const float inv = 1.0f / Ls;
      const float f0 = __shfl(f, l >> 3) * inv, f1 = __shfl(f, 8 + (l >> 3)) * inv;
      float ov[4];
#pragma unroll
      for (int n = 0; n < 4; ++n) { float t = asf(o0[n]) * f0 + asf(o1[n]) * f1; t += dppf<ROR8>(t); ov[n] = xrow_sum(t); }
      if (l < 8) *(LAS u32x2*)(L + O_O + l * 8) = u32x2{pack2(ov[0], ov[1]), pack2(ov[2], ov[3])};
    }
  } else {
    const int hw = w - 4;
    __builtin_amdgcn_s_setprio(3);
    u32x4 x0 = *(const GAS u32x4*)(x + l * 16), x1 = *(const GAS u32x4*)(x + 1024 + l * 16);
    u32x2 xw = *(const GAS u32x2*)(x + (l >> 5) * 1024 + (128 * hw + 4 * (l & 31)) * 2);
    cfence();
    u32x4 wqr[4];
#pragma unroll
    for (int i = 0; i < 4; ++i) {
      const int row = 128 * hw + 32 * i + (l >> 1);
      wqr[i] = ldnt((const GAS unsigned char*)wq_ + ((size_t)row * 4096 + kvh * 512 + j * 16 + (l & 1) * 8) * 2);
    }
    u64 E = ld_sc1((const GAS u64*)(ws + WS_EPOCH));
    cfence();
    u32x4 wa[8];
    {
      const __amdgpu_buffer_rsrc_t rw = __builtin_amdgcn_make_buffer_rsrc((void*)(hw == 3 ? w3_ : w1_), 0, DM * FF * 2, 0x00020000);
      const int wofs = (hw >= 2 && ffn) ? ((rb * 32 + 8 * g) * FF + cb * 128 + (l & 15) * 8) * 2 : OOB;
#pragma unroll
      for (int t = 0; t < 8; ++t) wa[t] = __builtin_amdgcn_raw_buffer_load_b128(rw, wofs, t * FF * 2, AUX_NT);
    }
    cfence();
    asm volatile("s_barrier" ::: "memory");
    asm volatile("" : "+v"(x0), "+v"(x1), "+v"(xw));
    {
      float s0 = 0.f, s1 = 0.f;
#pragma unroll
      for (int i = 0; i < 4; ++i) { s0 += blo(x0[i]) + bhi(x0[i]); s1 += blo(x1[i]) + bhi(x1[i]); }
      const float mean0 = wave_sum(s0) * (1.0f / DM), mean1 = wave_sum(s1) * (1.0f / DM);
      float q0 = 0.f, q1 = 0.f;
#pragma unroll
      for (int i = 0; i < 4; ++i) {
        float d;
        d = blo(x0[i]) - mean0; q0 += d * d; d = bhi(x0[i]) - mean0; q0 += d * d;
        d = blo(x1[i]) - mean1; q1 += d * d; d = bhi(x1[i]) - mean1; q1 += d * d;
      }
      const float var0 = wave_sum(q0) * (1.0f / DM), var1 = wave_sum(q1) * (1.0f / DM);
      const float mb = rbf(l < 32 ? mean0 : mean1);
      const float den = rbf(sqrtf(rbf(rbf(l < 32 ? var0 : var1) + 1e-5f)));
      *(LAS u32x2*)(L + O_XN + (l >> 5) * 1024 + (128 * hw + 4 * (l & 31)) * 2) =
          u32x2{pack2(rbf(blo(xw[0]) - mb) / den, rbf(bhi(xw[0]) - mb) / den), pack2(rbf(blo(xw[1]) - mb) / den, rbf(bhi(xw[1]) - mb) / den)};
    }
#pragma unroll
    for (int i = 0; i < 4; ++i) *(LAS u32x4*)(L + O_WQ + hw * 4096 + i * 1024 + l * 16) = wqr[i];
    lds_wait();
    LAS float* part = (LAS float*)(L + O_PART);
    {
      f32x4 dq = {0, 0, 0, 0};
#pragma unroll
      for (int s = 0; s < 4; ++s) {
        const u32x4 af = *(LAS const u32x4*)(L + O_XN + (l & 1) * 1024 + (128 * hw + 32 * s + 8 * g) * 2);
        const int ro = (32 * s + 8 * g + qq) * 32 + p4 * 8;
        const u32x2 b0 = tr_read(L + O_WQ + hw * 4096 + ro), b1 = tr_read(L + O_WQ + hw * 4096 + ro + 128);
        dq = mfma16(af, u32x4{b0[0], b0[1], b1[0], b1[1]}, dq);
      }
      if (l < 16) { part[hw * 32 + l] = dq[0]; part[hw * 32 + 16 + l] = dq[1]; }
    }
    asm volatile("" : "+v"(E));
    lds_arrive(cnt, l);
    const u64 T = E + 1;
    if (hw == 0) {
      lds_wait_cnt(cnt, 4);
      if (l < 32) {
        const float s = (part[l] + part[32 + l]) + (part[64 + l] + part[96 + l]);
        st_sc1((GAS u64*)(ws + WS_QBOX) + ((l >> 4) * 8 + kvh) * 512 + j * 16 + (l & 15), (T << 16) | (pack2(s, s) & 0xffffu));
      }
      cfence();
      lds_arrive(cnt + 2, l);
    } else if (hw == 1) {
      lds_wait_cnt(cnt + 4, 4);
      lds_wait_cnt(cnt, 4);
      if (l == 0) st_sc1((GAS u64*)(ws + WS_FLAG) + bid, T);
    } else if (ffn) {
      lds_wait_cnt(cnt, 4);
      const u32x4 xf = *(LAS const u32x4*)(L + O_XN + (l & 1) * 1024 + (rb * 32 + 8 * g) * 2);
      f32x4 r[4];
#pragma unroll
      for (int e = 0; e < 8; ++e) {
        const int q2 = e >> 1;
        const unsigned sel = (e & 1) ? 0x07060302u : 0x05040100u;
        const u32x4 fa = {__builtin_amdgcn_perm(wa[1][q2], wa[0][q2], sel), __builtin_amdgcn_perm(wa[3][q2], wa[2][q2], sel),
                          __builtin_amdgcn_perm(wa[5][q2], wa[4][q2], sel), __builtin_amdgcn_perm(wa[7][q2], wa[6][q2], sel)};
        const f32x4 da = mfma16(xf, fa, f32x4{0, 0, 0, 0});
        r[e >> 2][e & 3] = da[0]; r[2 + (e >> 2)][e & 3] = da[1];
      }
      LAS unsigned char* gu = L + O_GU + (hw - 2) * 1280;
      if (l < 16) {
#pragma unroll
        for (int i = 0; i < 4; ++i) *(LAS f32x4*)(gu + l * 80 + i * 16) = r[i];
      }
      lds_wait();
      const f32x4 gv = *(LAS f32x4*)(gu + (l >> 2) * 80 + (l & 3) * 16);
      lds_wait_cnt(cnt + 4, 4);
      ST16(san4(gv), myin + IN_GU + (hw - 2) * 1024 + l * 16);
    }
    while (__builtin_amdgcn_s_memrealtime() - t_entry < QPOLL_T) __builtin_amdgcn_s_sleep(2);
    {
      const int qoff = WS_QBOX + (gid * 512 + (tid - 256) * 2) * 8;
      const unsigned thi = (unsigned)(T >> 16), tlo = (unsigned)T & 0xffffu;
      u32x4 qg = LD16(qoff);
      cfence();
      lds_arrive(cnt + 3, l);
      asm volatile("" : "+v"(qg));
      unsigned spins = 0;
      while (qg[1] != thi || qg[3] != thi || (qg[0] >> 16) != tlo || (qg[2] >> 16) != tlo) {
        if (++spins > SPIN_CAP) break;
        asm volatile("" ::: "memory");
        qg = LD16(qoff);
      }
      *(LAS unsigned*)(L + O_Q + (tid - 256) * 4) = (qg[0] & 0xffffu) | (qg[2] << 16);
      lds_arrive(cnt + 1, l);
    }
    if (hw == 0) {
      u32x4 w2r[7];
#pragma unroll
      for (int i = 0; i < 7; ++i) w2r[i] = ldnt((const GAS unsigned char*)w2_ + ((size_t)(56 * j + 8 * i + (l >> 3)) * 512 + 64 * kvh + (l & 7) * 8) * 2);
      cfence();
      {
        const unsigned tl = (unsigned)T, th = (unsigned)(T >> 32);
        unsigned spins = 0;
        for (;;) {
          asm volatile("" ::: "memory");
          const u32x4 f0 = LD16(WS_FLAG + l * 32), f1 = LD16(WS_FLAG + l * 32 + 16);
          const bool ok = f0[0] == tl && f0[1] == th && f0[2] == tl && f0[3] == th && f1[0] == tl && f1[1] == th && f1[2] == tl && f1[3] == th;
          if (__all(ok) || ++spins > SPIN_CAP) break;
        }
        cfence();
      }
      lds_arrive(cnt + 7, l);
#pragma unroll
      for (int i = 0; i < 7; ++i) *(LAS u32x4*)(L + O_W2 + (8 * i + (l >> 3)) * W2_STR + (l & 7) * 16) = w2r[i];
      if (ffn) {
        const int po = WS_IN + ffn_wg(cb * 16 + (l >> 2)) * IN_STR + IN_GU + rb * 64 + (l & 3) * 16;
        u32x4 g0, g1;
        unsigned spins = 0;
        for (;;) {
          asm volatile("" ::: "memory");
          g0 = LD16(po); g1 = LD16(po + 1024);
          if (__all(ok4(g0) && ok4(g1)) || ++spins > SPIN_CAP) break;
          __builtin_amdgcn_s_sleep(8);
        }
        float hv[4];
#pragma unroll
        for (int n = 0; n < 4; ++n) {
          float tg = asf(g0[n]), tu = asf(g1[n]);
          tg += dppf<ROR4>(tg); tg += dppf<ROR8>(tg); tg = rbf(xrow_sum(tg));
          tu += dppf<ROR4>(tu); tu += dppf<ROR8>(tu); tu = rbf(xrow_sum(tu));
          hv[n] = rbf(silu(tg)) * tu;
        }
        u32x4 hp;
#pragma unroll
        for (int n = 0; n < 4; ++n) { const unsigned u = pack2(hv[n], dppf<XOR2>(hv[n])); hp[n] = u == INVAL ? 0x7FC07FC0u : u; }
        if (l < 2) ST16(hp, WS_HB + fs * 32 + l * 16);
      }
      {
        u32x4 hg;
        unsigned spins = 0;
        for (;;) {
          asm volatile("" ::: "memory");
          hg = LD16(l < 14 ? WS_HB + j * 224 + l * 16 : OOB);
          if (__all(ok4(hg)) || ++spins > SPIN_CAP) break;
          __builtin_amdgcn_s_sleep(8);
        }
        if (l < 16) {
          *(LAS u32x2*)(L + O_H + l * 8) = u32x2{__builtin_amdgcn_perm(hg[1], hg[0], 0x05040100u), __builtin_amdgcn_perm(hg[3], hg[2], 0x05040100u)};
          *(LAS u32x2*)(L + O_H + 128 + l * 8) = u32x2{__builtin_amdgcn_perm(hg[1], hg[0], 0x07060302u), __builtin_amdgcn_perm(hg[3], hg[2], 0x07060302u)};
        }
        lds_wait();
        f32x4 o0, o1;
#pragma unroll
        for (int n = 0; n < 4; ++n) {
          f32x4 d = {0, 0, 0, 0};
#pragma unroll
          for (int ks = 0; ks < 2; ++ks) {
            const u32x4 af = *(LAS const u32x4*)(L + O_H + (l & 1) * 128 + (32 * ks + 8 * g) * 2);
            const int r0 = min(32 * ks + 8 * g + qq, 51);
            const int ro = r0 * W2_STR + (16 * n + 4 * p4) * 2;
            const u32x2 b0 = tr_read(L + O_W2 + ro), b1 = tr_read(L + O_W2 + ro + 4 * W2_STR);
            d = mfma16(af, u32x4{b0[0], b0[1], b1[0], b1[1]}, d);
          }
          o0[n] = d[0]; o1[n] = d[1];
        }
        if (l < 16) { *(LAS f32x4*)(L + O_F + l * 16) = o0; *(LAS f32x4*)(L + O_F + 256 + l * 16) = o1; }
      }
      while (__builtin_amdgcn_readfirstlane(__hip_atomic_load(cnt + 6, __ATOMIC_RELAXED, __HIP_MEMORY_SCOPE_WORKGROUP)) < 3) __builtin_amdgcn_s_sleep(8);
      if (l < 32) ST16(san4(*(LAS f32x4*)(L + O_F + l * 16)), myin + IN_F2 + l * 16);
      if (bid == 0 && l == 0) st_sc1((GAS u64*)(ws + WS_EPOCH), T);
    } else if (hw == 1) {
      const GAS unsigned char* wb = (const GAS unsigned char*)wo_ + ((size_t)(akvh * 512 + 32 * asp) * 512 + l * 8) * 2;
#pragma unroll 1
      for (int h = 0; h < 2; ++h) {
        u32x4 wr[16];
#pragma unroll
        for (int i = 0; i < 16; ++i) wr[i] = ldnt(wb + (size_t)(h * 16 + i) * 1024);
        cfence();
#pragma unroll
        for (int i = 0; i < 16; ++i) *(LAS u32x4*)(L + O_WO + (h * 16 + i) * WO_STR + l * 16) = wr[i];
      }
      lds_arrive(cnt + 8, l);
    } else if (hw == 2) {
      xv = (float)((const GAS __bf16*)x_)[b * 512 + 64 * (cw >> 4) + 16 * (l & 3) + (cw & 15)];
      asm volatile("" : "+v"(xv));
    }
    __builtin_amdgcn_s_setprio(0);
  }
  lds_wait_cnt(cnt + 8, 1);
  u32x4 wf[4];
#pragma unroll
  for (int n = 0; n < 4; ++n) {
    const int ro = (8 * g + qq) * WO_STR + (64 * w + 16 * n + 4 * p4) * 2;
    const u32x2 b0 = tr_read(L + O_WO + ro), b1 = tr_read(L + O_WO + ro + 4 * WO_STR);
    wf[n] = u32x4{b0[0], b0[1], b1[0], b1[1]};
  }
  wg_barrier();
  {
    const u32x4 af = *(LAS const u32x4*)(L + O_O + g * 16);
    f32x4 so;
#pragma unroll
    for (int n = 0; n < 4; ++n) so[n] = mfma16(af, wf[n], f32x4{0, 0, 0, 0})[0];
    if (l < 16) ST16(san4(so), myin + IN_H2 + (16 * w + l) * 16);
  }
  if (w == 7) {
    const int fo = l < 32 ? WS_IN + ((l << 3) | kvh) * IN_STR + IN_F2 + (b * 16 + sp) * 16 : OOB;
    u32x4 fp = LD16(fo);
    unsigned spins = 0;
    while (!__all(ok4(fp)) && ++spins <= SPIN_CAP) {
      __builtin_amdgcn_s_sleep(4);
      asm volatile("" ::: "memory");
      const bool k0 = ok4(fp);
      const u32x4 n0 = LD16(k0 ? OOB : fo);
      fp = k0 ? fp : n0;
    }
    float f[4];
#pragma unroll
    for (int n = 0; n < 4; ++n) f[n] = wave_sum(asf(fp[n]));
    const float fv = l == 0 ? f[0] : l == 1 ? f[1] : l == 2 ? f[2] : f[3];
    if (l < 4) ((LAS float*)(L + O_FG))[l] = rbf(silu(rbf(fv)));
    lds_arrive(cnt + 5, l);
  }
  if (w == 6) {
    const int a0 = WS_IN + (b * 128 + l) * IN_STR + IN_H2 + cw * 16, a1 = a0 + 64 * IN_STR;
    u32x4 s0 = {INVAL, INVAL, INVAL, INVAL}, s1 = s0;
    u32x4 pa0 = LD16(a0), pa1 = LD16(a1);
    __builtin_amdgcn_s_sleep(PSTAG);
    for (unsigned spins = 0; spins <= SPIN_CAP; ++spins) {
      asm volatile("" ::: "memory");
      const bool k0 = ok4(s0), k1 = ok4(s1);
      const u32x4 pb0 = LD16(k0 ? OOB : a0), pb1 = LD16(k1 ? OOB : a1);
      cfence();
      s0 = k0 ? s0 : pa0; s1 = k1 ? s1 : pa1;
      const bool j0 = ok4(s0), j1 = ok4(s1);
      if (__all(j0 && j1)) break;
      asm volatile("" ::: "memory");
      pa0 = LD16(j0 ? OOB : a0); pa1 = LD16(j1 ? OOB : a1);
      cfence();
      s0 = j0 ? s0 : pb0; s1 = j1 ? s1 : pb1;
      if (__all(ok4(s0) && ok4(s1))) break;
    }
    float f[4];
#pragma unroll
    for (int n = 0; n < 4; ++n) f[n] = wave_sum(asf(s0[n]) + asf(s1[n]));
    const float s = l == 0 ? f[0] : l == 1 ? f[1] : l == 2 ? f[2] : f[3];
    lds_wait_cnt(cnt + 5, 1);
    if (l < 4) ((GAS __bf16*)out_)[b * 512 + 64 * (cw >> 4) + 16 * l + (cw & 15)] = (__bf16)(xv + rbf(rbf(s) * ((LAS float*)(L + O_FG))[l]));
  }
}

extern "C" void kernel_launch(void* const* d_in, const int* in_sizes, int n_in, void* d_out, int out_size, void* d_ws, size_t ws_size,
                              hipStream_t stream) {
  k_fused<<<NWG, NTHR, 0, stream>>>((const __hip_bfloat16*)d_in[0], (const __hip_bfloat16*)d_in[1], (const __hip_bfloat16*)d_in[2],
                                    (const __hip_bfloat16*)d_in[3], (const __hip_bfloat16*)d_in[4], (const __hip_bfloat16*)d_in[6],
                                    (const __hip_bfloat16*)d_in[7], (const __hip_bfloat16*)d_in[8], (unsigned char*)d_ws,
                                    (__hip_bfloat16*)d_out);
}
```

```cpp
#include <hip/hip_runtime.h>
#include <hip/hip_bf16.h>
#include <stdint.h>

#define GAS __attribute__((address_space(1)))
#define LAS __attribute__((address_space(3)))
typedef unsigned u32x4 __attribute__((ext_vector_type(4)));
typedef unsigned u32x2 __attribute__((ext_vector_type(2)));
typedef float f32x4 __attribute__((ext_vector_type(4)));
typedef short s16x8 __attribute__((ext_vector_type(8)));
typedef short s16x4 __attribute__((ext_vector_type(4)));
typedef __bf16 bf16x2_t __attribute__((ext_vector_type(2)));
typedef unsigned long long u64;
typedef GAS u64 gu64;

constexpr int DM = 512, SK = 32768, FF = 1792;
constexpr int NWG = 256, NTHR = 512, NSPLIT = 16;
constexpr int KEYS_WG = SK / NSPLIT;
constexpr int NTILE = KEYS_WG / (32 * 4);
constexpr float SCALE = 0.08838834764831845f;
constexpr float LOG2E = 1.4426950408889634f;
constexpr unsigned SPIN_CAP = 1u << 15;
constexpr unsigned INVAL = 0xFFFFFFFFu;
constexpr unsigned QPOLL_T = 460;
constexpr int PSTAG = 8;
constexpr int SHIFT = 2;
constexpr int TOUCH_0 = 300, TOUCH_D = 200, TOUCH_S = 8;
constexpr int OOB = 0x7ff00000;

constexpr int WS_EPOCH = 0;
constexpr int WS_FLAG = 2048;
constexpr int WS_QBOX = 8192;
constexpr int WS_HB = WS_QBOX + 65536;
constexpr int WS_IN = WS_HB + 8192;
constexpr int IN_H1 = 0;
constexpr int IN_H2 = 2048;
constexpr int IN_GU = 4096;
constexpr int IN_F2 = 6144;
constexpr int IN_ML = 6656;
constexpr int IN_STR = 7168;
constexpr int WS_END = WS_IN + NWG * IN_STR;

constexpr int WO_STR = 1056;
constexpr int O_XN = 0;
constexpr int O_WQ = 2048;
constexpr int W2_STR = 160;
constexpr int O_W2 = O_WQ + 16384;
constexpr int O_GU = O_W2 + 56 * W2_STR + 64;
constexpr int O_PART = O_GU + 2 * 16 * 80;
constexpr int O_H = O_PART + 512;
constexpr int O_Q = O_H + 256;
constexpr int O_WO = O_Q + 1024;
constexpr int O_REC = O_WO + 32 * WO_STR;
constexpr int O_F = O_REC + 8192;
constexpr int O_MLW = O_F + 512;
constexpr int O_O = O_MLW + 128;
constexpr int O_FG = O_O + 64;
constexpr int O_CNT = O_FG + 16;
constexpr int LDS_BYTES = 84 * 1024;
static_assert(O_CNT + 48 <= LDS_BYTES, "");

#define RLX_AGENT __ATOMIC_RELAXED, __HIP_MEMORY_SCOPE_AGENT
__device__ __forceinline__ float asf(unsigned u) { return __builtin_bit_cast(float, u); }
__device__ __forceinline__ unsigned asu(float f) { return __builtin_bit_cast(unsigned, f); }
__device__ __forceinline__ unsigned san(float f) { const unsigned u = asu(f); return u == INVAL ? 0x7FC00000u : u; }
__device__ __forceinline__ u32x4 san4(f32x4 v) { return u32x4{san(v[0]), san(v[1]), san(v[2]), san(v[3])}; }
__device__ __forceinline__ bool ok4(u32x4 v) { return v[0] != INVAL && v[1] != INVAL && v[2] != INVAL && v[3] != INVAL; }
__device__ __forceinline__ unsigned pack2(float lo, float hi) {
  bf16x2_t v = {(__bf16)lo, (__bf16)hi};
  return __builtin_bit_cast(unsigned, v);
}
__device__ __forceinline__ float rbf(float x) { return (float)(__bf16)x; }
__device__ __forceinline__ float blo(unsigned u) { return asf(u << 16); }
__device__ __forceinline__ float bhi(unsigned u) { return asf(u & 0xffff0000u); }
__device__ __forceinline__ f32x4 mfma16(u32x4 a, u32x4 b, f32x4 c) {
  return __builtin_amdgcn_mfma_f32_16x16x32_bf16(__builtin_bit_cast(s16x8, a), __builtin_bit_cast(s16x8, b), c, 0, 0, 0);
}
template <int CTRL> __device__ __forceinline__ float dppf(float x) {
  return asf(__builtin_amdgcn_mov_dpp(asu(x), CTRL, 0xf, 0xf, true));
}
template <int CTRL> __device__ __forceinline__ unsigned dppu(unsigned x) {
  return __builtin_amdgcn_mov_dpp(x, CTRL, 0xf, 0xf, true);
}
template <int CTRL, int BANK> __device__ __forceinline__ float updf(float old, float x) {
  return asf(__builtin_amdgcn_update_dpp(asu(old), asu(x), CTRL, 0xf, BANK, false));
}
constexpr int ROR4 = 0x124, ROR8 = 0x128, ROR12 = 0x12C, QID = 0xE4, XOR1 = 0xB1, XOR2 = 0x4E;
__device__ __forceinline__ float xrow_max(float x) {
  auto s = __builtin_amdgcn_permlane16_swap(asu(x), asu(x), false, false);
  x = fmaxf(asf(s[0]), asf(s[1]));
  auto t = __builtin_amdgcn_permlane32_swap(asu(x), asu(x), false, false);
  return fmaxf(asf(t[0]), asf(t[1]));
}
__device__ __forceinline__ float xrow_sum(float x) {
  auto s = __builtin_amdgcn_permlane16_swap(asu(x), asu(x), false, false);
  x = asf(s[0]) + asf(s[1]);
  auto t = __builtin_amdgcn_permlane32_swap(asu(x), asu(x), false, false);
  return asf(t[0]) + asf(t[1]);
}
__device__ __forceinline__ float wave_sum(float x) {
  x += dppf<XOR1>(x); x += dppf<XOR2>(x); x += dppf<ROR4>(x); x += dppf<ROR8>(x);
  return xrow_sum(x);
}
__device__ __forceinline__ u32x2 tr_read(LAS const unsigned char* p) {
  return __builtin_bit_cast(u32x2, __builtin_amdgcn_ds_read_tr16_b64_v4i16((LAS s16x4*)p));
}
__device__ __forceinline__ float silu(float x) { return x / (1.0f + __expf(-x)); }
__device__ __forceinline__ void wg_barrier() { asm volatile("s_waitcnt lgkmcnt(0)\n\ts_barrier" ::: "memory"); }
__device__ __forceinline__ void lds_wait() { asm volatile("s_waitcnt lgkmcnt(0)" ::: "memory"); }
__device__ __forceinline__ void cfence() { asm volatile("" ::: "memory"); __builtin_amdgcn_sched_barrier(0); }
__device__ __forceinline__ u32x4 ldnt(const GAS unsigned char* p) { return __builtin_nontemporal_load((const GAS u32x4*)p); }
__device__ __forceinline__ u64 ld_sc1(const GAS u64* p) { return __hip_atomic_load((gu64*)p, RLX_AGENT); }
__device__ __forceinline__ void st_sc1(GAS u64* p, u64 v) { __hip_atomic_store(p, v, RLX_AGENT); }
constexpr int AUX_SC1 = 16, AUX_NT = 2;
#define LD16(off) __builtin_amdgcn_raw_buffer_load_b128(rs, (off), 0, AUX_SC1)
#define LD8(off) __builtin_amdgcn_raw_buffer_load_b64(rs, (off), 0, AUX_SC1)
#define ST16(v, off) __builtin_amdgcn_raw_buffer_store_b128((v), rs, (off), 0, AUX_SC1)
#define ST8(v, off) __builtin_amdgcn_raw_buffer_store_b64((v), rs, (off), 0, AUX_SC1)
__device__ __forceinline__ void lds_arrive(LAS unsigned* c, int l) {
  asm volatile("s_waitcnt lgkmcnt(0)" ::: "memory");
  if (l == 0) __hip_atomic_fetch_add(c, 1u, __ATOMIC_RELAXED, __HIP_MEMORY_SCOPE_WORKGROUP);
}
__device__ __forceinline__ void lds_wait_cnt(LAS unsigned* c, unsigned n) {
  while (__builtin_amdgcn_readfirstlane(__hip_atomic_load(c, __ATOMIC_RELAXED, __HIP_MEMORY_SCOPE_WORKGROUP)) < n) __builtin_amdgcn_s_sleep(1);
  asm volatile("" ::: "memory");
}
__device__ __forceinline__ int ffn_wg(int fs) { return ((fs % 28) << 3) | (fs / 28); }

__device__ __forceinline__ float qk_reduce(const f32x4 d0, const f32x4 d1, const f32x4 d2, const f32x4 d3) {
  float z0 = d0[0];
  z0 = updf<QID, 0x2>(z0, d1[1]); z0 = updf<QID, 0x4>(z0, d2[2]); z0 = updf<QID, 0x8>(z0, d3[3]);
  float z1 = dppf<ROR4>(d0[3]);
  z1 = updf<ROR4, 0x2>(z1, d1[0]); z1 = updf<ROR4, 0x4>(z1, d2[1]); z1 = updf<ROR4, 0x8>(z1, d3[2]);
  float z2 = dppf<ROR8>(d0[2]);
  z2 = updf<ROR8, 0x2>(z2, d1[3]); z2 = updf<ROR8, 0x4>(z2, d2[0]); z2 = updf<ROR8, 0x8>(z2, d3[1]);
  float z3 = dppf<ROR12>(d0[1]);
  z3 = updf<ROR12, 0x2>(z3, d1[2]); z3 = updf<ROR12, 0x4>(z3, d2[3]); z3 = updf<ROR12, 0x8>(z3, d3[0]);
  return (z0 + z1) + (z2 + z3);
}


__global__ void __launch_bounds__(NTHR, 2)
k_fused(const __hip_bfloat16* __restrict__ x_, const __hip_bfloat16* __restrict__ k_, const __hip_bfloat16* __restrict__ v_,
        const __hip_bfloat16* __restrict__ wq_, const __hip_bfloat16* __restrict__ wo_, const __hip_bfloat16* __restrict__ w1_,
        const __hip_bfloat16* __restrict__ w3_, const __hip_bfloat16* __restrict__ w2_, unsigned char* ws_,
        __hip_bfloat16* __restrict__ out_) {
  __shared__ __attribute__((aligned(16))) unsigned char lds[LDS_BYTES];
  LAS unsigned char* L = (LAS unsigned char*)lds;
  LAS unsigned* cnt = (LAS unsigned*)(L + O_CNT);
  GAS unsigned char* ws = (GAS unsigned char*)ws_;
  const u64 t_entry = __builtin_amdgcn_s_memrealtime();
  const int bid = blockIdx.x, kvh = bid & 7, j = bid >> 3, b = j >> 4, sp = j & 15;
  const int gid = bid >> 4, akvh = gid & 7, asp = bid & 15;
  const int tid = threadIdx.x, l = tid & 63, w = __builtin_amdgcn_readfirstlane(tid >> 6);
  const int g = l >> 4, qq = (l >> 2) & 3, p4 = l & 3;
  const int cw = kvh * 16 + sp;
  const int myin = WS_IN + bid * IN_STR;
  const bool ffn = j < 28;
  const int fs = kvh * 28 + (ffn ? j : 0);
  const int rb = fs & 15, cb = fs >> 4;
  const __amdgpu_buffer_rsrc_t rs = __builtin_amdgcn_make_buffer_rsrc((void*)ws_, 0, WS_END, 0x00020000);
  const GAS unsigned char* x = (const GAS unsigned char*)x_;
  float xv = 0.f;
  if (w < 4) {
    const size_t kvbase = (size_t)gid * SK * 256;
    const __amdgpu_buffer_rsrc_t rk = __builtin_amdgcn_make_buffer_rsrc((void*)((const unsigned char*)k_ + kvbase), 0, SK * 256, 0x00020000);
    const __amdgpu_buffer_rsrc_t rv = __builtin_amdgcn_make_buffer_rsrc((void*)((const unsigned char*)v_ + kvbase), 0, SK * 256, 0x00020000);
    const int so0 = (asp * 64 + w) * 8192;
    const int koff = (qq * 16 + g * 4 + p4) * 16, voff = l * 16;
    u32x4 K[2][8], V[2][8];
#define LOADK(c_, so_) _Pragma("unroll") for (int t = 0; t < 8; ++t) K[c_][t] = __builtin_amdgcn_raw_buffer_load_b128(rk, koff + t * 1024, (so_), AUX_NT)
#define LOADV(c_, so_) _Pragma("unroll") for (int t = 0; t < 8; ++t) V[c_][t] = __builtin_amdgcn_raw_buffer_load_b128(rv, voff + t * 1024, (so_), AUX_NT)
    ST16((u32x4{INVAL, INVAL, INVAL, INVAL}), myin + w * 2048 + l * 16);
    ST16((u32x4{INVAL, INVAL, INVAL, INVAL}), w < 3 ? myin + w * 2048 + 1024 + l * 16 : (ffn && l < 2) ? WS_HB + fs * 32 + l * 16 : OOB);
    if (tid < 3) *(LAS u32x4*)(cnt + 4 * tid) = u32x4{0u, 0u, 0u, 0u};
    cfence();
    LOADK(0, so0);
    cfence();
    wg_barrier();
    asm volatile("s_waitcnt vmcnt(8)" ::: "memory");
    lds_arrive(cnt + 4, l);
    lds_wait_cnt(cnt + 2, 1);
    LOADV(0, so0);
    LOADK(1, so0 + 4 * 8192);
    cfence();
    lds_wait_cnt(cnt + 3, 4);
    LOADV(1, so0 + 4 * 8192);
    cfence();
    lds_wait_cnt(cnt + 1, 4);
    const u32x4 qf = *(LAS const u32x4*)(L + O_Q + (p4 * 128 + (4 * g + qq) * 8) * 2);
    f32x4 acc[8];
#pragma unroll
    for (int e = 0; e < 8; ++e) acc[e] = f32x4{0, 0, 0, 0};
    float m = -INFINITY, lsum = 0.f;
    int nx = 2;
    const int role = (bid & 1) ? 1 : 2;
    bool valid[2] = {true, true};
#pragma unroll 1
    do {
#pragma unroll
      for (int c = 0; c < 2; ++c) {
        f32x4 d[8];
#pragma unroll
        for (int t = 0; t < 8; ++t) d[t] = mfma16(K[c][t], qf, f32x4{0, 0, 0, 0});
        cfence();
        const bool cur = valid[c];
        int so = so0 + nx * (4 * 8192);
        if (nx >= NTILE - 1) {
          valid[c] = nx == NTILE - 1 ? !(role == 1 && w >= 4 - SHIFT) : (nx == NTILE && role == 2 && w < SHIFT);
          if (nx == NTILE) so = ((asp ^ 1) * 64 + 64 - SHIFT + w) * 8192;
          if (!valid[c]) so = OOB;
        }
        LOADK(c, so);
        cfence();
        float S0 = qk_reduce(d[0], d[1], d[2], d[3]), S1 = qk_reduce(d[4], d[5], d[6], d[7]);
        if (!cur) S0 = S1 = -INFINITY;
        unsigned pk = pack2(S0, S1);
        pk = pack2(blo(pk) * SCALE, bhi(pk) * SCALE);
        const float a0 = blo(pk), a1 = bhi(pk);
        float tm = fmaxf(a0, a1);
        tm = fmaxf(tm, dppf<ROR4>(tm)); tm = fmaxf(tm, dppf<ROR8>(tm)); tm = xrow_max(tm);
        if (__any(tm > m)) {
          const float mn = fmaxf(m, tm);
          const float al = __builtin_amdgcn_exp2f((m - mn) * LOG2E);
          m = mn; lsum *= al;
          const float al0 = dppf<0x00>(al), al1 = dppf<0x55>(al), al2 = dppf<0xAA>(al), al3 = dppf<0xFF>(al);
#pragma unroll
          for (int e = 0; e < 8; ++e) { acc[e][0] *= al0; acc[e][1] *= al1; acc[e][2] *= al2; acc[e][3] *= al3; }
        }
        const float p0 = __builtin_amdgcn_exp2f((a0 - m) * LOG2E), p1 = __builtin_amdgcn_exp2f((a1 - m) * LOG2E);
        lsum += p0 + p1;
        const unsigned pw = pack2(p0, p1);
        const u32x4 pf = {pw, dppu<ROR12>(pw), dppu<ROR8>(pw), dppu<ROR4>(pw)};
#pragma unroll
        for (int e = 0; e < 8; ++e) {
          const int r = e >> 1;
          const unsigned sel = (e & 1) ? 0x07060302u : 0x05040100u;
          const u32x4 bf = {__builtin_amdgcn_perm(V[c][4][r], V[c][0][r], sel), __builtin_amdgcn_perm(V[c][5][r], V[c][1][r], sel),
                            __builtin_amdgcn_perm(V[c][6][r], V[c][2][r], sel), __builtin_amdgcn_perm(V[c][7][r], V[c][3][r], sel)};
          acc[e] = mfma16(pf, bf, acc[e]);
        }
        cfence();
        LOADV(c, so);
        cfence();
        ++nx;
      }
    } while (valid[0] || valid[1]);
    lsum += dppf<ROR4>(lsum); lsum += dppf<ROR8>(lsum); lsum = xrow_sum(lsum);
    LAS float* rec = (LAS float*)(L + O_REC) + w * 512;
    LAS float* ml = (LAS float*)(L + O_MLW);
    if (l < 16) {
#pragma unroll
      for (int hh = 0; hh < 4; ++hh) {
        *(LAS f32x4*)(rec + hh * 128 + 8 * l) = f32x4{acc[0][hh], acc[1][hh], acc[2][hh], acc[3][hh]};
        *(LAS f32x4*)(rec + hh * 128 + 8 * l + 4) = f32x4{acc[4][hh], acc[5][hh], acc[6][hh], acc[7][hh]};
      }
      if (l < 4) { ml[w * 8 + l * 2] = m; ml[w * 8 + l * 2 + 1] = lsum; }
    }
    lds_wait();
    unsigned arr = 0;
    if (l == 0) arr = __hip_atomic_fetch_add(cnt + 6, 1u, __ATOMIC_RELAXED, __HIP_MEMORY_SCOPE_WORKGROUP);
    if (__builtin_amdgcn_readfirstlane(arr) == 3) {
      lds_wait_cnt(cnt + 7, 1);
#pragma unroll
      for (int i = 0; i < 2; ++i) {
        const int hh = 2 * i + (l >> 5);
        float M = -INFINITY;
#pragma unroll
        for (int ww = 0; ww < 4; ++ww) M = fmaxf(M, ml[ww * 8 + hh * 2]);
        f32x4 o = {0, 0, 0, 0};
        float Ls = 0.f;
#pragma unroll
        for (int ww = 0; ww < 4; ++ww) {
          const float f = __builtin_amdgcn_exp2f((ml[ww * 8 + hh * 2] - M) * LOG2E);
          o += f * *(LAS f32x4*)(L + O_REC + ww * 2048 + i * 1024 + l * 16);
          Ls += f * ml[ww * 8 + hh * 2 + 1];
        }
        const int cin = WS_IN + (gid * 16 + 8 * i + (l >> 3)) * IN_STR;
        ST16(san4(o), cin + IN_H1 + asp * 128 + (l & 7) * 16);
        ST8((u32x2{san(M), san(Ls)}), (l & 7) == 0 ? cin + IN_ML + asp * 8 : OOB);
      }
      const int a0 = myin + IN_H1 + l * 16, a1 = a0 + 1024, a2 = myin + IN_ML + (l & 15) * 8;
      u32x4 o0 = {INVAL, INVAL, INVAL, INVAL}, o1 = o0;
      u32x2 mlg = {INVAL, INVAL};
      u32x4 pa0 = LD16(a0), pa1 = LD16(a1);
      u32x2 pa2 = LD8(a2);
      __builtin_amdgcn_s_sleep(PSTAG);
      for (unsigned spins = 0; spins <= SPIN_CAP; ++spins) {
        asm volatile("" ::: "memory");
        bool k0 = ok4(o0), k1 = ok4(o1), k2 = mlg[0] != INVAL && mlg[1] != INVAL;
        const u32x4 pb0 = LD16(k0 ? OOB : a0), pb1 = LD16(k1 ? OOB : a1);
        const u32x2 pb2 = LD8(k2 ? OOB : a2);
        cfence();
        o0 = k0 ? o0 : pa0; o1 = k1 ? o1 : pa1; mlg = k2 ? mlg : pa2;
        const bool j0 = ok4(o0), j1 = ok4(o1), j2 = mlg[0] != INVAL && mlg[1] != INVAL;
        if (__all(j0 && j1 && j2)) break;
        asm volatile("" ::: "memory");
        pa0 = LD16(j0 ? OOB : a0); pa1 = LD16(j1 ? OOB : a1); pa2 = LD8(j2 ? OOB : a2);
        cfence();
        o0 = j0 ? o0 : pb0; o1 = j1 ? o1 : pb1; mlg = j2 ? mlg : pb2;
        if (__all(ok4(o0) && ok4(o1) && mlg[0] != INVAL && mlg[1] != INVAL)) break;
      }
      const float mp = asf(mlg[0]), lp = asf(mlg[1]);
      float M = mp;
      M = fmaxf(M, dppf<XOR1>(M)); M = fmaxf(M, dppf<XOR2>(M)); M = fmaxf(M, dppf<ROR4>(M)); M = fmaxf(M, dppf<ROR8>(M));
      const float f = __builtin_amdgcn_exp2f((mp - M) * LOG2E);
      float Ls = f * lp;
      Ls += dppf<XOR1>(Ls); Ls += dppf<XOR2>(Ls); Ls += dppf<ROR4>(Ls); Ls += dppf<ROR8>(Ls);
      const float inv = 1.0f / Ls;
      const float f0 = __shfl(f, l >> 3) * inv, f1 = __shfl(f, 8 + (l >> 3)) * inv;
      float ov[4];
#pragma unroll
      for (int n = 0; n < 4; ++n) { float t = asf(o0[n]) * f0 + asf(o1[n]) * f1; t += dppf<ROR8>(t); ov[n] = xrow_sum(t); }
      if (l < 8) *(LAS u32x2*)(L + O_O + l * 8) = u32x2{pack2(ov[0], ov[1]), pack2(ov[2], ov[3])};
    }
  } else {
    const int hw = w - 4;
    __builtin_amdgcn_s_setprio(3);
    u32x4 x0 = *(const GAS u32x4*)(x + l * 16), x1 = *(const GAS u32x4*)(x + 1024 + l * 16);
    u32x2 xw = *(const GAS u32x2*)(x + (l >> 5) * 1024 + (128 * hw + 4 * (l & 31)) * 2);
    cfence();
    u32x4 wqr[4];
#pragma unroll
    for (int i = 0; i < 4; ++i) {
      const int row = 128 * hw + 32 * i + (l >> 1);
      wqr[i] = ldnt((const GAS unsigned char*)wq_ + ((size_t)row * 4096 + kvh * 512 + j * 16 + (l & 1) * 8) * 2);
    }
    u64 E = ld_sc1((const GAS u64*)(ws + WS_EPOCH));
    cfence();
    u32x4 wa[8];
    {
      const __amdgpu_buffer_rsrc_t rw = __builtin_amdgcn_make_buffer_rsrc((void*)(hw == 3 ? w3_ : w1_), 0, DM * FF * 2, 0x00020000);
      const int wofs = (hw >= 2 && ffn) ? ((rb * 32 + 8 * g) * FF + cb * 128 + (l & 15) * 8) * 2 : OOB;
#pragma unroll
      for (int t = 0; t < 8; ++t) wa[t] = __builtin_amdgcn_raw_buffer_load_b128(rw, wofs, t * FF * 2, AUX_NT);
    }
    cfence();
    asm volatile("s_barrier" ::: "memory");
    asm volatile("" : "+v"(x0), "+v"(x1), "+v"(xw));
    {
      float s0 = 0.f, s1 = 0.f;
#pragma unroll
      for (int i = 0; i < 4; ++i) { s0 += blo(x0[i]) + bhi(x0[i]); s1 += blo(x1[i]) + bhi(x1[i]); }
      const float mean0 = wave_sum(s0) * (1.0f / DM), mean1 = wave_sum(s1) * (1.0f / DM);
      float q0 = 0.f, q1 = 0.f;
#pragma unroll
      for (int i = 0; i < 4; ++i) {
        float d;
        d = blo(x0[i]) - mean0; q0 += d * d; d = bhi(x0[i]) - mean0; q0 += d * d;
        d = blo(x1[i]) - mean1; q1 += d * d; d = bhi(x1[i]) - mean1; q1 += d * d;
      }
      const float var0 = wave_sum(q0) * (1.0f / DM), var1 = wave_sum(q1) * (1.0f / DM);
      const float mb = rbf(l < 32 ? mean0 : mean1);
      const float den = rbf(sqrtf(rbf(rbf(l < 32 ? var0 : var1) + 1e-5f)));
      *(LAS u32x2*)(L + O_XN + (l >> 5) * 1024 + (128 * hw + 4 * (l & 31)) * 2) =
          u32x2{pack2(rbf(blo(xw[0]) - mb) / den, rbf(bhi(xw[0]) - mb) / den), pack2(rbf(blo(xw[1]) - mb) / den, rbf(bhi(xw[1]) - mb) / den)};
    }
#pragma unroll
    for (int i = 0; i < 4; ++i) *(LAS u32x4*)(L + O_WQ + hw * 4096 + i * 1024 + l * 16) = wqr[i];
    lds_wait();
    LAS float* part = (LAS float*)(L + O_PART);
    {
      f32x4 dq = {0, 0, 0, 0};
#pragma unroll
      for (int s = 0; s < 4; ++s) {
        const u32x4 af = *(LAS const u32x4*)(L + O_XN + (l & 1) * 1024 + (128 * hw + 32 * s + 8 * g) * 2);
        const int ro = (32 * s + 8 * g + qq) * 32 + p4 * 8;
        const u32x2 b0 = tr_read(L + O_WQ + hw * 4096 + ro), b1 = tr_read(L + O_WQ + hw * 4096 + ro + 128);
        dq = mfma16(af, u32x4{b0[0], b0[1], b1[0], b1[1]}, dq);
      }
      if (l < 16) { part[hw * 32 + l] = dq[0]; part[hw * 32 + 16 + l] = dq[1]; }
    }
    asm volatile("" : "+v"(E));
    lds_arrive(cnt, l);
    const u64 T = E + 1;
    if (hw == 0) {
      lds_wait_cnt(cnt, 4);
      if (l < 32) {
        const float s = (part[l] + part[32 + l]) + (part[64 + l] + part[96 + l]);
        st_sc1((GAS u64*)(ws + WS_QBOX) + ((l >> 4) * 8 + kvh) * 512 + j * 16 + (l & 15), (T << 16) | (pack2(s, s) & 0xffffu));
      }
      cfence();
      lds_arrive(cnt + 2, l);
    } else if (hw == 1) {
      lds_wait_cnt(cnt + 4, 4);
      lds_wait_cnt(cnt, 4);
      if (l == 0) st_sc1((GAS u64*)(ws + WS_FLAG) + bid, T);
    } else if (ffn) {
      lds_wait_cnt(cnt, 4);
      const u32x4 xf = *(LAS const u32x4*)(L + O_XN + (l & 1) * 1024 + (rb * 32 + 8 * g) * 2);
      f32x4 r[4];
#pragma unroll
      for (int e = 0; e < 8; ++e) {
        const int q2 = e >> 1;
        const unsigned sel = (e & 1) ? 0x07060302u : 0x05040100u;
        const u32x4 fa = {__builtin_amdgcn_perm(wa[1][q2], wa[0][q2], sel), __builtin_amdgcn_perm(wa[3][q2], wa[2][q2], sel),
                          __builtin_amdgcn_perm(wa[5][q2], wa[4][q2], sel), __builtin_amdgcn_perm(wa[7][q2], wa[6][q2], sel)};
        const f32x4 da = mfma16(xf, fa, f32x4{0, 0, 0, 0});
        r[e >> 2][e & 3] = da[0]; r[2 + (e >> 2)][e & 3] = da[1];
      }
      LAS unsigned char* gu = L + O_GU + (hw - 2) * 1280;
      if (l < 16) {
#pragma unroll
        for (int i = 0; i < 4; ++i) *(LAS f32x4*)(gu + l * 80 + i * 16) = r[i];
      }
      lds_wait();
      const f32x4 gv = *(LAS f32x4*)(gu + (l >> 2) * 80 + (l & 3) * 16);
      lds_wait_cnt(cnt + 4, 4);
      ST16(san4(gv), myin + IN_GU + (hw - 2) * 1024 + l * 16);
    }
    while (__builtin_amdgcn_s_memrealtime() - t_entry < QPOLL_T) __builtin_amdgcn_s_sleep(2);
    {
      const int qoff = WS_QBOX + (gid * 512 + (tid - 256) * 2) * 8;
      const unsigned thi = (unsigned)(T >> 16), tlo = (unsigned)T & 0xffffu;
      u32x4 qg = LD16(qoff);
      cfence();
      lds_arrive(cnt + 3, l);
      asm volatile("" : "+v"(qg));
      unsigned spins = 0;
      while (qg[1] != thi || qg[3] != thi || (qg[0] >> 16) != tlo || (qg[2] >> 16) != tlo) {
        if (++spins > SPIN_CAP) break;
        asm volatile("" ::: "memory");
        qg = LD16(qoff);
      }
      *(LAS unsigned*)(L + O_Q + (tid - 256) * 4) = (qg[0] & 0xffffu) | (qg[2] << 16);
      lds_arrive(cnt + 1, l);
    }
    if (hw == 0) {
      u32x4 w2r[7];
#pragma unroll
      for (int i = 0; i < 7; ++i) w2r[i] = ldnt((const GAS unsigned char*)w2_ + ((size_t)(56 * j + 8 * i + (l >> 3)) * 512 + 64 * kvh + (l & 7) * 8) * 2);
      cfence();
      {
        const unsigned tl = (unsigned)T, th = (unsigned)(T >> 32);
        unsigned spins = 0;
        for (;;) {
          asm volatile("" ::: "memory");
          const u32x4 f0 = LD16(WS_FLAG + l * 32), f1 = LD16(WS_FLAG + l * 32 + 16);
          const bool ok = f0[0] == tl && f0[1] == th && f0[2] == tl && f0[3] == th && f1[0] == tl && f1[1] == th && f1[2] == tl && f1[3] == th;
          if (__all(ok) || ++spins > SPIN_CAP) break;
        }
        cfence();
      }
      lds_arrive(cnt + 7, l);
#pragma unroll
      for (int i = 0; i < 7; ++i) *(LAS u32x4*)(L + O_W2 + (8 * i + (l >> 3)) * W2_STR + (l & 7) * 16) = w2r[i];
      if (ffn) {
        const int po = WS_IN + ffn_wg(cb * 16 + (l >> 2)) * IN_STR + IN_GU + rb * 64 + (l & 3) * 16;
        u32x4 g0, g1;
        unsigned spins = 0;
        for (;;) {
          asm volatile("" ::: "memory");
          g0 = LD16(po); g1 = LD16(po + 1024);
          if (__all(ok4(g0) && ok4(g1)) || ++spins > SPIN_CAP) break;
          __builtin_amdgcn_s_sleep(8);
        }
        float hv[4];
#pragma unroll
        for (int n = 0; n < 4; ++n) {
          float tg = asf(g0[n]), tu = asf(g1[n]);
          tg += dppf<ROR4>(tg); tg += dppf<ROR8>(tg); tg = rbf(xrow_sum(tg));
          tu += dppf<ROR4>(tu); tu += dppf<ROR8>(tu); tu = rbf(xrow_sum(tu));
          hv[n] = rbf(silu(tg)) * tu;
        }
        u32x4 hp;
#pragma unroll
        for (int n = 0; n < 4; ++n) { const unsigned u = pack2(hv[n], dppf<XOR2>(hv[n])); hp[n] = u == INVAL ? 0x7FC07FC0u : u; }
        if (l < 2) ST16(hp, WS_HB + fs * 32 + l * 16);
      }
      {
        u32x4 hg;
        unsigned spins = 0;
        for (;;) {
          asm volatile("" ::: "memory");
          hg = LD16(l < 14 ? WS_HB + j * 224 + l * 16 : OOB);
          if (__all(ok4(hg)) || ++spins > SPIN_CAP) break;
          __builtin_amdgcn_s_sleep(8);
        }
        if (l < 16) {
          *(LAS u32x2*)(L + O_H + l * 8) = u32x2{__builtin_amdgcn_perm(hg[1], hg[0], 0x05040100u), __builtin_amdgcn_perm(hg[3], hg[2], 0x05040100u)};
          *(LAS u32x2*)(L + O_H + 128 + l * 8) = u32x2{__builtin_amdgcn_perm(hg[1], hg[0], 0x07060302u), __builtin_amdgcn_perm(hg[3], hg[2], 0x07060302u)};
        }
        lds_wait();
        f32x4 o0, o1;
#pragma unroll
        for (int n = 0; n < 4; ++n) {
          f32x4 d = {0, 0, 0, 0};
#pragma unroll
          for (int ks = 0; ks < 2; ++ks) {
            const u32x4 af = *(LAS const u32x4*)(L + O_H + (l & 1) * 128 + (32 * ks + 8 * g) * 2);
            const int r0 = min(32 * ks + 8 * g + qq, 51);
            const int ro = r0 * W2_STR + (16 * n + 4 * p4) * 2;
            const u32x2 b0 = tr_read(L + O_W2 + ro), b1 = tr_read(L + O_W2 + ro + 4 * W2_STR);
            d = mfma16(af, u32x4{b0[0], b0[1], b1[0], b1[1]}, d);
          }
          o0[n] = d[0]; o1[n] = d[1];
        }
        if (l < 16) { *(LAS f32x4*)(L + O_F + l * 16) = o0; *(LAS f32x4*)(L + O_F + 256 + l * 16) = o1; }
      }
      while (__builtin_amdgcn_readfirstlane(__hip_atomic_load(cnt + 6, __ATOMIC_RELAXED, __HIP_MEMORY_SCOPE_WORKGROUP)) < 3) __builtin_amdgcn_s_sleep(8);
      if (l < 32) ST16(san4(*(LAS f32x4*)(L + O_F + l * 16)), myin + IN_F2 + l * 16);
      if (bid == 0 && l == 0) st_sc1((GAS u64*)(ws + WS_EPOCH), T);
    } else if (hw == 1) {
      const GAS unsigned char* wb = (const GAS unsigned char*)wo_ + ((size_t)(akvh * 512 + 32 * asp) * 512 + l * 8) * 2;
#pragma unroll 1
      for (int h = 0; h < 2; ++h) {
        u32x4 wr[16];
#pragma unroll
        for (int i = 0; i < 16; ++i) wr[i] = ldnt(wb + (size_t)(h * 16 + i) * 1024);
        cfence();
#pragma unroll
        for (int i = 0; i < 16; ++i) *(LAS u32x4*)(L + O_WO + (h * 16 + i) * WO_STR + l * 16) = wr[i];
      }
      lds_arrive(cnt + 8, l);
    } else if (hw == 3) {
      const size_t kvb = ((size_t)gid * SK + asp * KEYS_WG) * 256;
      const GAS unsigned char* tp = (const GAS unsigned char*)((l & 8) ? v_ : k_) + kvb + (l & 7) * 4096;
      unsigned sink = 0;
#pragma unroll 1
      for (int r = 2; r < NTILE; ++r) {
        while (__builtin_amdgcn_s_memrealtime() - t_entry < (unsigned)(TOUCH_0 + (r - 2) * TOUCH_D)) __builtin_amdgcn_s_sleep(8);
        if (l < 16 && ((l & 7) % TOUCH_S) == 0) sink += __builtin_nontemporal_load((const GAS unsigned*)(tp + (size_t)r * 32768));
      }
      asm volatile("" :: "v"(sink));
    } else if (hw == 2) {
      xv = (float)((const GAS __bf16*)x_)[b * 512 + 64 * (cw >> 4) + 16 * (l & 3) + (cw & 15)];
      asm volatile("" : "+v"(xv));
    }
    __builtin_amdgcn_s_setprio(0);
  }
  lds_wait_cnt(cnt + 8, 1);
  u32x4 wf[4];
#pragma unroll
  for (int n = 0; n < 4; ++n) {
    const int ro = (8 * g + qq) * WO_STR + (64 * w + 16 * n + 4 * p4) * 2;
    const u32x2 b0 = tr_read(L + O_WO + ro), b1 = tr_read(L + O_WO + ro + 4 * WO_STR);
    wf[n] = u32x4{b0[0], b0[1], b1[0], b1[1]};
  }
  wg_barrier();
  {
    const u32x4 af = *(LAS const u32x4*)(L + O_O + g * 16);
    f32x4 so;
#pragma unroll
    for (int n = 0; n < 4; ++n) so[n] = mfma16(af, wf[n], f32x4{0, 0, 0, 0})[0];
    if (l < 16) ST16(san4(so), myin + IN_H2 + (16 * w + l) * 16);
  }
  if (w == 7) {
    const int fo = l < 32 ? WS_IN + ((l << 3) | kvh) * IN_STR + IN_F2 + (b * 16 + sp) * 16 : OOB;
    u32x4 fp = LD16(fo);
    unsigned spins = 0;
    while (!__all(ok4(fp)) && ++spins <= SPIN_CAP) {
      __builtin_amdgcn_s_sleep(4);
      asm volatile("" ::: "memory");
      const bool k0 = ok4(fp);
      const u32x4 n0 = LD16(k0 ? OOB : fo);
      fp = k0 ? fp : n0;
    }
    float f[4];
#pragma unroll
    for (int n = 0; n < 4; ++n) f[n] = wave_sum(asf(fp[n]));
    const float fv = l == 0 ? f[0] : l == 1 ? f[1] : l == 2 ? f[2] : f[3];
    if (l < 4) ((LAS float*)(L + O_FG))[l] = rbf(silu(rbf(fv)));
    lds_arrive(cnt + 5, l);
  }
  if (w == 6) {
    const int a0 = WS_IN + (b * 128 + l) * IN_STR + IN_H2 + cw * 16, a1 = a0 + 64 * IN_STR;
    u32x4 s0 = {INVAL, INVAL, INVAL, INVAL}, s1 = s0;
    u32x4 pa0 = LD16(a0), pa1 = LD16(a1);
    __builtin_amdgcn_s_sleep(PSTAG);
    for (unsigned spins = 0; spins <= SPIN_CAP; ++spins) {
      asm volatile("" ::: "memory");
      const bool k0 = ok4(s0), k1 = ok4(s1);
      const u32x4 pb0 = LD16(k0 ? OOB : a0), pb1 = LD16(k1 ? OOB : a1);
      cfence();
      s0 = k0 ? s0 : pa0; s1 = k1 ? s1 : pa1;
      const bool j0 = ok4(s0), j1 = ok4(s1);
      if (__all(j0 && j1)) break;
      asm volatile("" ::: "memory");
      pa0 = LD16(j0 ? OOB : a0); pa1 = LD16(j1 ? OOB : a1);
      cfence();
      s0 = j0 ? s0 : pb0; s1 = j1 ? s1 : pb1;
      if (__all(ok4(s0) && ok4(s1))) break;
    }
    float f[4];
#pragma unroll
    for (int n = 0; n < 4; ++n) f[n] = wave_sum(asf(s0[n]) + asf(s1[n]));
    const float s = l == 0 ? f[0] : l == 1 ? f[1] : l == 2 ? f[2] : f[3];
    lds_wait_cnt(cnt + 5, 1);
    if (l < 4) ((GAS __bf16*)out_)[b * 512 + 64 * (cw >> 4) + 16 * l + (cw & 15)] = (__bf16)(xv + rbf(rbf(s) * ((LAS float*)(L + O_FG))[l]));
  }
}

extern "C" void kernel_launch(void* const* d_in, const int* in_sizes, int n_in, void* d_out, int out_size, void* d_ws, size_t ws_size,
                              hipStream_t stream) {
  k_fused<<<NWG, NTHR, 0, stream>>>((const __hip_bfloat16*)d_in[0], (const __hip_bfloat16*)d_in[1], (const __hip_bfloat16*)d_in[2],
                                    (const __hip_bfloat16*)d_in[3], (const __hip_bfloat16*)d_in[4], (const __hip_bfloat16*)d_in[6],
                                    (const __hip_bfloat16*)d_in[7], (const __hip_bfloat16*)d_in[8], (unsigned char*)d_ws,
                                    (__hip_bfloat16*)d_out);
}
```

```cpp
#include <hip/hip_runtime.h>
#include <hip/hip_bf16.h>
#include <stdint.h>

#define GAS __attribute__((address_space(1)))
#define LAS __attribute__((address_space(3)))
typedef unsigned u32x4 __attribute__((ext_vector_type(4)));
typedef unsigned u32x2 __attribute__((ext_vector_type(2)));
typedef float f32x4 __attribute__((ext_vector_type(4)));
typedef short s16x8 __attribute__((ext_vector_type(8)));
typedef short s16x4 __attribute__((ext_vector_type(4)));
typedef __bf16 bf16x2_t __attribute__((ext_vector_type(2)));
typedef unsigned long long u64;
typedef GAS u64 gu64;

constexpr int DM = 512, SK = 32768, FF = 1792;
constexpr int NWG = 256, NTHR = 512, NSPLIT = 16;
constexpr int KEYS_WG = SK / NSPLIT;
constexpr int NTILE = KEYS_WG / (32 * 4);
constexpr float SCALE = 0.08838834764831845f;
constexpr float LOG2E = 1.4426950408889634f;
constexpr unsigned SPIN_CAP = 1u << 15;
constexpr unsigned INVAL = 0xFFFFFFFFu;
constexpr unsigned QPOLL_T = 460;
constexpr int PSTAG = 8;
constexpr int SHIFT = 2;
constexpr int OOB = 0x7ff00000;

constexpr int WS_EPOCH = 0;
constexpr int WS_FLAG = 2048;
constexpr int WS_QBOX = 8192;
constexpr int WS_HB = WS_QBOX + 65536;
constexpr int WS_IN = WS_HB + 8192;
constexpr int IN_H1 = 0;
constexpr int IN_H2 = 2048;
constexpr int IN_GU = 4096;
constexpr int IN_F2 = 6144;
constexpr int IN_ML = 6656;
constexpr int IN_STR = 7168;
constexpr int WS_END = WS_IN + NWG * IN_STR;

constexpr int WO_STR = 1056;
constexpr int O_XN = 0;
constexpr int O_WQ = 2048;
constexpr int W2_STR = 160;
constexpr int O_W2 = O_WQ + 16384;
constexpr int O_GU = O_W2 + 56 * W2_STR + 64;
constexpr int O_PART = O_GU + 2 * 16 * 80;
constexpr int O_H = O_PART + 512;
constexpr int O_Q = O_H + 256;
constexpr int O_WO = O_Q + 1024;
constexpr int O_REC = O_WO + 32 * WO_STR;
constexpr int O_F = O_REC + 8192;
constexpr int O_MLW = O_F + 512;
constexpr int O_O = O_MLW + 128;
constexpr int O_FG = O_O + 64;
constexpr int O_CNT = O_FG + 16;
constexpr int LDS_BYTES = 84 * 1024;

#define RLX_AGENT __ATOMIC_RELAXED, __HIP_MEMORY_SCOPE_AGENT
__device__ __forceinline__ float asf(unsigned u) { return __builtin_bit_cast(float, u); }
__device__ __forceinline__ unsigned asu(float f) { return __builtin_bit_cast(unsigned, f); }
__device__ __forceinline__ unsigned san(float f) { const unsigned u = asu(f); return u == INVAL ? 0x7FC00000u : u; }
__device__ __forceinline__ u32x4 san4(f32x4 v) { return u32x4{san(v[0]), san(v[1]), san(v[2]), san(v[3])}; }
__device__ __forceinline__ bool ok4(u32x4 v) { return v[0] != INVAL && v[1] != INVAL && v[2] != INVAL && v[3] != INVAL; }
__device__ __forceinline__ unsigned pack2(float lo, float hi) {
  bf16x2_t v = {(__bf16)lo, (__bf16)hi};
  return __builtin_bit_cast(unsigned, v);
}
__device__ __forceinline__ float rbf(float x) { return (float)(__bf16)x; }
__device__ __forceinline__ float blo(unsigned u) { return asf(u << 16); }
__device__ __forceinline__ float bhi(unsigned u) { return asf(u & 0xffff0000u); }
__device__ __forceinline__ f32x4 mfma16(u32x4 a, u32x4 b, f32x4 c) {
  return __builtin_amdgcn_mfma_f32_16x16x32_bf16(__builtin_bit_cast(s16x8, a), __builtin_bit_cast(s16x8, b), c, 0, 0, 0);
}
template <int CTRL> __device__ __forceinline__ float dppf(float x) {
  return asf(__builtin_amdgcn_mov_dpp(asu(x), CTRL, 0xf, 0xf, true));
}
template <int CTRL> __device__ __forceinline__ unsigned dppu(unsigned x) {
  return __builtin_amdgcn_mov_dpp(x, CTRL, 0xf, 0xf, true);
}
template <int CTRL, int BANK> __device__ __forceinline__ float updf(float old, float x) {
  return asf(__builtin_amdgcn_update_dpp(asu(old), asu(x), CTRL, 0xf, BANK, false));
}
constexpr int ROR4 = 0x124, ROR8 = 0x128, ROR12 = 0x12C, QID = 0xE4, XOR1 = 0xB1, XOR2 = 0x4E;
__device__ __forceinline__ float xrow_max(float x) {
  auto s = __builtin_amdgcn_permlane16_swap(asu(x), asu(x), false, false);
  x = fmaxf(asf(s[0]), asf(s[1]));
  auto t = __builtin_amdgcn_permlane32_swap(asu(x), asu(x), false, false);
  return fmaxf(asf(t[0]), asf(t[1]));
}
__device__ __forceinline__ float xrow_sum(float x) {
  auto s = __builtin_amdgcn_permlane16_swap(asu(x), asu(x), false, false);
  x = asf(s[0]) + asf(s[1]);
  auto t = __builtin_amdgcn_permlane32_swap(asu(x), asu(x), false, false);
  return asf(t[0]) + asf(t[1]);
}
__device__ __forceinline__ float wave_sum(float x) {
  x += dppf<XOR1>(x); x += dppf<XOR2>(x); x += dppf<ROR4>(x); x += dppf<ROR8>(x);
  return xrow_sum(x);
}
__device__ __forceinline__ u32x2 tr_read(LAS const unsigned char* p) {
  return __builtin_bit_cast(u32x2, __builtin_amdgcn_ds_read_tr16_b64_v4i16((LAS s16x4*)p));
}
__device__ __forceinline__ float silu(float x) { return x / (1.0f + __expf(-x)); }
__device__ __forceinline__ void wg_barrier() { asm volatile("s_waitcnt lgkmcnt(0)\n\ts_barrier" ::: "memory"); }
__device__ __forceinline__ void lds_wait() { asm volatile("s_waitcnt lgkmcnt(0)" ::: "memory"); }
__device__ __forceinline__ void cfence() { asm volatile("" ::: "memory"); __builtin_amdgcn_sched_barrier(0); }
__device__ __forceinline__ u32x4 ldnt(const GAS unsigned char* p) { return __builtin_nontemporal_load((const GAS u32x4*)p); }
__device__ __forceinline__ u64 ld_sc1(const GAS u64* p) { return __hip_atomic_load((gu64*)p, RLX_AGENT); }
__device__ __forceinline__ void st_sc1(GAS u64* p, u64 v) { __hip_atomic_store(p, v, RLX_AGENT); }
constexpr int AUX_SC1 = 16, AUX_NT = 2;
#define LD16(off) __builtin_amdgcn_raw_buffer_load_b128(rs, (off), 0, AUX_SC1)
#define LD8(off) __builtin_amdgcn_raw_buffer_load_b64(rs, (off), 0, AUX_SC1)
#define ST16(v, off) __builtin_amdgcn_raw_buffer_store_b128((v), rs, (off), 0, AUX_SC1)
#define ST8(v, off) __builtin_amdgcn_raw_buffer_store_b64((v), rs, (off), 0, AUX_SC1)
__device__ __forceinline__ void lds_arrive(LAS unsigned* c, int l) {
  asm volatile("s_waitcnt lgkmcnt(0)" ::: "memory");
  if (l == 0) __hip_atomic_fetch_add(c, 1u, __ATOMIC_RELAXED, __HIP_MEMORY_SCOPE_WORKGROUP);
}
__device__ __forceinline__ void lds_wait_cnt(LAS unsigned* c, unsigned n) {
  while (__builtin_amdgcn_readfirstlane(__hip_atomic_load(c, __ATOMIC_RELAXED, __HIP_MEMORY_SCOPE_WORKGROUP)) < n) __builtin_amdgcn_s_sleep(1);
  asm volatile("" ::: "memory");
}
__device__ __forceinline__ int ffn_wg(int fs) { return ((fs % 28) << 3) | (fs / 28); }

__device__ __forceinline__ float qk_reduce(const f32x4 d0, const f32x4 d1, const f32x4 d2, const f32x4 d3) {
  float z0 = d0[0];
  z0 = updf<QID, 0x2>(z0, d1[1]); z0 = updf<QID, 0x4>(z0, d2[2]); z0 = updf<QID, 0x8>(z0, d3[3]);
  float z1 = dppf<ROR4>(d0[3]);
  z1 = updf<ROR4, 0x2>(z1, d1[0]); z1 = updf<ROR4, 0x4>(z1, d2[1]); z1 = updf<ROR4, 0x8>(z1, d3[2]);
  float z2 = dppf<ROR8>(d0[2]);
  z2 = updf<ROR8, 0x2>(z2, d1[3]); z2 = updf<ROR8, 0x4>(z2, d2[0]); z2 = updf<ROR8, 0x8>(z2, d3[1]);
  float z3 = dppf<ROR12>(d0[1]);
  z3 = updf<ROR12, 0x2>(z3, d1[2]); z3 = updf<ROR12, 0x4>(z3, d2[3]); z3 = updf<ROR12, 0x8>(z3, d3[0]);
  return (z0 + z1) + (z2 + z3);
}


__global__ void __launch_bounds__(NTHR, 2)
k_fused(const __hip_bfloat16* __restrict__ x_, const __hip_bfloat16* __restrict__ k_, const __hip_bfloat16* __restrict__ v_,
        const __hip_bfloat16* __restrict__ wq_, const __hip_bfloat16* __restrict__ wo_, const __hip_bfloat16* __restrict__ w1_,
        const __hip_bfloat16* __restrict__ w3_, const __hip_bfloat16* __restrict__ w2_, unsigned char* ws_,
        __hip_bfloat16* __restrict__ out_) {
  __shared__ __attribute__((aligned(16))) unsigned char lds[LDS_BYTES];
  LAS unsigned char* L = (LAS unsigned char*)lds;
  LAS unsigned* cnt = (LAS unsigned*)(L + O_CNT);
  GAS unsigned char* ws = (GAS unsigned char*)ws_;
  const u64 t_entry = __builtin_amdgcn_s_memrealtime();
  const int bid = blockIdx.x, kvh = bid & 7, j = bid >> 3, b = j >> 4, sp = j & 15;
  const int gid = bid >> 4, akvh = gid & 7, asp = bid & 15;
  const int tid = threadIdx.x, l = tid & 63, w = __builtin_amdgcn_readfirstlane(tid >> 6);
  const int g = l >> 4, qq = (l >> 2) & 3, p4 = l & 3;
  const int cw = kvh * 16 + sp;
  const int myin = WS_IN + bid * IN_STR;
  const bool ffn = j < 28;
  const int fs = kvh * 28 + (ffn ? j : 0);
  const int rb = fs & 15, cb = fs >> 4;
  const __amdgpu_buffer_rsrc_t rs = __builtin_amdgcn_make_buffer_rsrc((void*)ws_, 0, WS_END, 0x00020000);
  const GAS unsigned char* x = (const GAS unsigned char*)x_;
  float xv = 0.f;
  if (w < 4) {
    const size_t kvbase = (size_t)gid * SK * 256;
    const __amdgpu_buffer_rsrc_t rk = __builtin_amdgcn_make_buffer_rsrc((void*)((const unsigned char*)k_ + kvbase), 0, SK * 256, 0x00020000);
    const __amdgpu_buffer_rsrc_t rv = __builtin_amdgcn_make_buffer_rsrc((void*)((const unsigned char*)v_ + kvbase), 0, SK * 256, 0x00020000);
    const int so0 = (asp * 64 + w) * 8192;
    const int koff = (qq * 16 + g * 4 + p4) * 16, voff = l * 16;
    u32x4 K[2][8], V[2][8];
#define LOADK(c_, so_) _Pragma("unroll") for (int t = 0; t < 8; ++t) K[c_][t] = __builtin_amdgcn_raw_buffer_load_b128(rk, koff + t * 1024, (so_), AUX_NT)
#define LOADV(c_, so_) _Pragma("unroll") for (int t = 0; t < 8; ++t) V[c_][t] = __builtin_amdgcn_raw_buffer_load_b128(rv, voff + t * 1024, (so_), AUX_NT)
    ST16((u32x4{INVAL, INVAL, INVAL, INVAL}), myin + w * 2048 + l * 16);
    ST16((u32x4{INVAL, INVAL, INVAL, INVAL}), w < 3 ? myin + w * 2048 + 1024 + l * 16 : (ffn && l < 2) ? WS_HB + fs * 32 + l * 16 : OOB);
    if (tid < 3) *(LAS u32x4*)(cnt + 4 * tid) = u32x4{0u, 0u, 0u, 0u};
    cfence();
    LOADK(0, so0);
    cfence();
    wg_barrier();
    asm volatile("s_waitcnt vmcnt(8)" ::: "memory");
    lds_arrive(cnt + 4, l);
    lds_wait_cnt(cnt + 2, 1);
    LOADV(0, so0);
    LOADK(1, so0 + 4 * 8192);
    cfence();
    lds_wait_cnt(cnt + 3, 4);
    LOADV(1, so0 + 4 * 8192);
    cfence();
    lds_wait_cnt(cnt + 1, 4);
    const u32x4 qf = *(LAS const u32x4*)(L + O_Q + (p4 * 128 + (4 * g + qq) * 8) * 2);
    f32x4 acc[8];
#pragma unroll
    for (int e = 0; e < 8; ++e) acc[e] = f32x4{0, 0, 0, 0};
    float m = -INFINITY, lsum = 0.f;
    int nx = 2;
    const int role = (bid & 1) ? 1 : 2;
    bool valid[2] = {true, true};
#pragma unroll 1
    do {
#pragma unroll
      for (int c = 0; c < 2; ++c) {
        f32x4 d[8];
#pragma unroll
        for (int t = 0; t < 8; ++t) d[t] = mfma16(K[c][t], qf, f32x4{0, 0, 0, 0});
        cfence();
        const bool cur = valid[c];
        int so = so0 + nx * (4 * 8192);
        if (nx >= NTILE - 1) {
          valid[c] = nx == NTILE - 1 ? !(role == 1 && w >= 4 - SHIFT) : (nx == NTILE && role == 2 && w < SHIFT);
          if (nx == NTILE) so = ((asp ^ 1) * 64 + 64 - SHIFT + w) * 8192;
          if (!valid[c]) so = OOB;
        }
        LOADK(c, so);
        cfence();
        float S0 = qk_reduce(d[0], d[1], d[2], d[3]), S1 = qk_reduce(d[4], d[5], d[6], d[7]);
        if (!cur) S0 = S1 = -INFINITY;
        unsigned pk = pack2(S0, S1);
        pk = pack2(blo(pk) * SCALE, bhi(pk) * SCALE);
        const float a0 = blo(pk), a1 = bhi(pk);
        float tm = fmaxf(a0, a1);
        tm = fmaxf(tm, dppf<ROR4>(tm)); tm = fmaxf(tm, dppf<ROR8>(tm)); tm = xrow_max(tm);
        if (__any(tm > m)) {
          const float mn = fmaxf(m, tm);
          const float al = __builtin_amdgcn_exp2f((m - mn) * LOG2E);
          m = mn; lsum *= al;
          const float al0 = dppf<0x00>(al), al1 = dppf<0x55>(al), al2 = dppf<0xAA>(al), al3 = dppf<0xFF>(al);
#pragma unroll
          for (int e = 0; e < 8; ++e) { acc[e][0] *= al0; acc[e][1] *= al1; acc[e][2] *= al2; acc[e][3] *= al3; }
        }
        const float p0 = __builtin_amdgcn_exp2f((a0 - m) * LOG2E), p1 = __builtin_amdgcn_exp2f((a1 - m) * LOG2E);
        lsum += p0 + p1;
        const unsigned pw = pack2(p0, p1);
        const u32x4 pf = {pw, dppu<ROR12>(pw), dppu<ROR8>(pw), dppu<ROR4>(pw)};
#pragma unroll
        for (int e = 0; e < 8; ++e) {
          const int r = e >> 1;
          const unsigned sel = (e & 1) ? 0x07060302u : 0x05040100u;
          const u32x4 bf = {__builtin_amdgcn_perm(V[c][4][r], V[c][0][r], sel), __builtin_amdgcn_perm(V[c][5][r], V[c][1][r], sel),
                            __builtin_amdgcn_perm(V[c][6][r], V[c][2][r], sel), __builtin_amdgcn_perm(V[c][7][r], V[c][3][r], sel)};
          acc[e] = mfma16(pf, bf, acc[e]);
        }
        cfence();
        LOADV(c, so);
        cfence();
        ++nx;
      }
    } while (valid[0] || valid[1]);
    lsum += dppf<ROR4>(lsum); lsum += dppf<ROR8>(lsum); lsum = xrow_sum(lsum);
    LAS float* rec = (LAS float*)(L + O_REC) + w * 512;
    LAS float* ml = (LAS float*)(L + O_MLW);
    if (l < 16) {
#pragma unroll
      for (int hh = 0; hh < 4; ++hh) {
        *(LAS f32x4*)(rec + hh * 128 + 8 * l) = f32x4{acc[0][hh], acc[1][hh], acc[2][hh], acc[3][hh]};
        *(LAS f32x4*)(rec + hh * 128 + 8 * l + 4) = f32x4{acc[4][hh], acc[5][hh], acc[6][hh], acc[7][hh]};
      }
      if (l < 4) { ml[w * 8 + l * 2] = m; ml[w * 8 + l * 2 + 1] = lsum; }
    }
    lds_wait();
    unsigned arr = 0;
    if (l == 0) arr = __hip_atomic_fetch_add(cnt + 6, 1u, __ATOMIC_RELAXED, __HIP_MEMORY_SCOPE_WORKGROUP);
    if (__builtin_amdgcn_readfirstlane(arr) == 3) {
      lds_wait_cnt(cnt + 7, 1);
#pragma unroll
      for (int i = 0; i < 2; ++i) {
        const int hh = 2 * i + (l >> 5);
        float M = -INFINITY;
#pragma unroll
        for (int ww = 0; ww < 4; ++ww) M = fmaxf(M, ml[ww * 8 + hh * 2]);
        f32x4 o = {0, 0, 0, 0};
        float Ls = 0.f;
#pragma unroll
        for (int ww = 0; ww < 4; ++ww) {
          const float f = __builtin_amdgcn_exp2f((ml[ww * 8 + hh * 2] - M) * LOG2E);
          o += f * *(LAS f32x4*)(L + O_REC + ww * 2048 + i * 1024 + l * 16);
          Ls += f * ml[ww * 8 + hh * 2 + 1];
        }
        const int cin = WS_IN + (gid * 16 + 8 * i + (l >> 3)) * IN_STR;
        ST16(san4(o), cin + IN_H1 + asp * 128 + (l & 7) * 16);
        ST8((u32x2{san(M), san(Ls)}), (l & 7) == 0 ? cin + IN_ML + asp * 8 : OOB);
      }
      const int a0 = myin + IN_H1 + l * 16, a1 = a0 + 1024, a2 = myin + IN_ML + (l & 15) * 8;
      u32x4 o0 = {INVAL, INVAL, INVAL, INVAL}, o1 = o0;
      u32x2 mlg = {INVAL, INVAL};
      u32x4 pa0 = LD16(a0), pa1 = LD16(a1);
      u32x2 pa2 = LD8(a2);
      __builtin_amdgcn_s_sleep(PSTAG);
      for (unsigned spins = 0; spins <= SPIN_CAP; ++spins) {
        asm volatile("" ::: "memory");
        bool k0 = ok4(o0), k1 = ok4(o1), k2 = mlg[0] != INVAL && mlg[1] != INVAL;
        const u32x4 pb0 = LD16(k0 ? OOB : a0), pb1 = LD16(k1 ? OOB : a1);
        const u32x2 pb2 = LD8(k2 ? OOB : a2);
        cfence();
        o0 = k0 ? o0 : pa0; o1 = k1 ? o1 : pa1; mlg = k2 ? mlg : pa2;
        const bool j0 = ok4(o0), j1 = ok4(o1), j2 = mlg[0] != INVAL && mlg[1] != INVAL;
        if (__all(j0 && j1 && j2)) break;
        asm volatile("" ::: "memory");
        pa0 = LD16(j0 ? OOB : a0); pa1 = LD16(j1 ? OOB : a1); pa2 = LD8(j2 ? OOB : a2);
        cfence();
        o0 = j0 ? o0 : pb0; o1 = j1 ? o1 : pb1; mlg = j2 ? mlg : pb2;
        if (__all(ok4(o0) && ok4(o1) && mlg[0] != INVAL && mlg[1] != INVAL)) break;
      }
      const float mp = asf(mlg[0]), lp = asf(mlg[1]);
      float M = mp;
      M = fmaxf(M, dppf<XOR1>(M)); M = fmaxf(M, dppf<XOR2>(M)); M = fmaxf(M, dppf<ROR4>(M)); M = fmaxf(M, dppf<ROR8>(M));
      const float f = __builtin_amdgcn_exp2f((mp - M) * LOG2E);
      float Ls = f * lp;
      Ls += dppf<XOR1>(Ls); Ls += dppf<XOR2>(Ls); Ls += dppf<ROR4>(Ls); Ls += dppf<ROR8>(Ls);
      const float inv = 1.0f / Ls;
      const float f0 = __shfl(f, l >> 3) * inv, f1 = __shfl(f, 8 + (l >> 3)) * inv;
      float ov[4];
#pragma unroll
      for (int n = 0; n < 4; ++n) { float t = asf(o0[n]) * f0 + asf(o1[n]) * f1; t += dppf<ROR8>(t); ov[n] = xrow_sum(t); }
      if (l < 8) *(LAS u32x2*)(L + O_O + l * 8) = u32x2{pack2(ov[0], ov[1]), pack2(ov[2], ov[3])};
    }
  } else {
    const int hw = w - 4;
    __builtin_amdgcn_s_setprio(3);
    u32x4 x0 = *(const GAS u32x4*)(x + l * 16), x1 = *(const GAS u32x4*)(x + 1024 + l * 16);
    u32x2 xw = *(const GAS u32x2*)(x + (l >> 5) * 1024 + (128 * hw + 4 * (l & 31)) * 2);
    cfence();
    u32x4 wqr[4];
#pragma unroll
    for (int i = 0; i < 4; ++i) {
      const int row = 128 * hw + 32 * i + (l >> 1);
      wqr[i] = ldnt((const GAS unsigned char*)wq_ + ((size_t)row * 4096 + kvh * 512 + j * 16 + (l & 1) * 8) * 2);
    }
    u64 E = ld_sc1((const GAS u64*)(ws + WS_EPOCH));
    cfence();
    u32x4 wa[8];
    {
      const __amdgpu_buffer_rsrc_t rw = __builtin_amdgcn_make_buffer_rsrc((void*)(hw == 3 ? w3_ : w1_), 0, DM * FF * 2, 0x00020000);
      const int wofs = (hw >= 2 && ffn) ? ((rb * 32 + 8 * g) * FF + cb * 128 + (l & 15) * 8) * 2 : OOB;
#pragma unroll
      for (int t = 0; t < 8; ++t) wa[t] = __builtin_amdgcn_raw_buffer_load_b128(rw, wofs, t * FF * 2, AUX_NT);
    }
    cfence();
    asm volatile("s_barrier" ::: "memory");
    asm volatile("" : "+v"(x0), "+v"(x1), "+v"(xw));
    {
      float s0 = 0.f, s1 = 0.f;
#pragma unroll
      for (int i = 0; i < 4; ++i) { s0 += blo(x0[i]) + bhi(x0[i]); s1 += blo(x1[i]) + bhi(x1[i]); }
      const float mean0 = wave_sum(s0) * (1.0f / DM), mean1 = wave_sum(s1) * (1.0f / DM);
      float q0 = 0.f, q1 = 0.f;
#pragma unroll
      for (int i = 0; i < 4; ++i) {
        float d;
        d = blo(x0[i]) - mean0; q0 += d * d; d = bhi(x0[i]) - mean0; q0 += d * d;
        d = blo(x1[i]) - mean1; q1 += d * d; d = bhi(x1[i]) - mean1; q1 += d * d;
      }
      const float var0 = wave_sum(q0) * (1.0f / DM), var1 = wave_sum(q1) * (1.0f / DM);
      const float mb = rbf(l < 32 ? mean0 : mean1);
      const float den = rbf(sqrtf(rbf(rbf(l < 32 ? var0 : var1) + 1e-5f)));
      *(LAS u32x2*)(L + O_XN + (l >> 5) * 1024 + (128 * hw + 4 * (l & 31)) * 2) =
          u32x2{pack2(rbf(blo(xw[0]) - mb) / den, rbf(bhi(xw[0]) - mb) / den), pack2(rbf(blo(xw[1]) - mb) / den, rbf(bhi(xw[1]) - mb) / den)};
    }
#pragma unroll
    for (int i = 0; i < 4; ++i) *(LAS u32x4*)(L + O_WQ + hw * 4096 + i * 1024 + l * 16) = wqr[i];
    lds_wait();
    LAS float* part = (LAS float*)(L + O_PART);
    {
      f32x4 dq = {0, 0, 0, 0};
#pragma unroll
      for (int s = 0; s < 4; ++s) {
        const u32x4 af = *(LAS const u32x4*)(L + O_XN + (l & 1) * 1024 + (128 * hw + 32 * s + 8 * g) * 2);
        const int ro = (32 * s + 8 * g + qq) * 32 + p4 * 8;
        const u32x2 b0 = tr_read(L + O_WQ + hw * 4096 + ro), b1 = tr_read(L + O_WQ + hw * 4096 + ro + 128);
        dq = mfma16(af, u32x4{b0[0], b0[1], b1[0], b1[1]}, dq);
      }
      if (l < 16) { part[hw * 32 + l] = dq[0]; part[hw * 32 + 16 + l] = dq[1]; }
    }
    asm volatile("" : "+v"(E));
    lds_arrive(cnt, l);
    const u64 T = E + 1;
    if (hw == 0) {
      lds_wait_cnt(cnt, 4);
      if (l < 32) {
        const float s = (part[l] + part[32 + l]) + (part[64 + l] + part[96 + l]);
        st_sc1((GAS u64*)(ws + WS_QBOX) + ((l >> 4) * 8 + kvh) * 512 + j * 16 + (l & 15), (T << 16) | (pack2(s, s) & 0xffffu));
      }
      cfence();
      lds_arrive(cnt + 2, l);
    } else if (hw == 1) {
      lds_wait_cnt(cnt + 4, 4);
      lds_wait_cnt(cnt, 4);
      if (l == 0) st_sc1((GAS u64*)(ws + WS_FLAG) + bid, T);
    } else if (ffn) {
      lds_wait_cnt(cnt, 4);
      const u32x4 xf = *(LAS const u32x4*)(L + O_XN + (l & 1) * 1024 + (rb * 32 + 8 * g) * 2);
      f32x4 r[4];
#pragma unroll
      for (int e = 0; e < 8; ++e) {
        const int q2 = e >> 1;
        const unsigned sel = (e & 1) ? 0x07060302u : 0x05040100u;
        const u32x4 fa = {__builtin_amdgcn_perm(wa[1][q2], wa[0][q2], sel), __builtin_amdgcn_perm(wa[3][q2], wa[2][q2], sel),
                          __builtin_amdgcn_perm(wa[5][q2], wa[4][q2], sel), __builtin_amdgcn_perm(wa[7][q2], wa[6][q2], sel)};
        const f32x4 da = mfma16(xf, fa, f32x4{0, 0, 0, 0});
        r[e >> 2][e & 3] = da[0]; r[2 + (e >> 2)][e & 3] = da[1];
      }
      LAS unsigned char* gu = L + O_GU + (hw - 2) * 1280;
      if (l < 16) {
#pragma unroll
        for (int i = 0; i < 4; ++i) *(LAS f32x4*)(gu + l * 80 + i * 16) = r[i];
      }
      lds_wait();
      const f32x4 gv = *(LAS f32x4*)(gu + (l >> 2) * 80 + (l & 3) * 16);
      lds_wait_cnt(cnt + 4, 4);
      ST16(san4(gv), myin + IN_GU + (hw - 2) * 1024 + l * 16);
    }
    while (__builtin_amdgcn_s_memrealtime() - t_entry < QPOLL_T) __builtin_amdgcn_s_sleep(2);
    {
      const int qoff = WS_QBOX + (gid * 512 + (tid - 256) * 2) * 8;
      const unsigned thi = (unsigned)(T >> 16), tlo = (unsigned)T & 0xffffu;
      u32x4 qg = LD16(qoff);
      cfence();
      lds_arrive(cnt + 3, l);
      asm volatile("" : "+v"(qg));
      unsigned spins = 0;
      while (qg[1] != thi || qg[3] != thi || (qg[0] >> 16) != tlo || (qg[2] >> 16) != tlo) {
        if (++spins > SPIN_CAP) break;
        asm volatile("" ::: "memory");
        qg = LD16(qoff);
      }
      *(LAS unsigned*)(L + O_Q + (tid - 256) * 4) = (qg[0] & 0xffffu) | (qg[2] << 16);
      lds_arrive(cnt + 1, l);
    }
    if (hw == 0) {
      u32x4 w2r[7];
#pragma unroll
      for (int i = 0; i < 7; ++i) w2r[i] = ldnt((const GAS unsigned char*)w2_ + ((size_t)(56 * j + 8 * i + (l >> 3)) * 512 + 64 * kvh + (l & 7) * 8) * 2);
      cfence();
      {
        const unsigned tl = (unsigned)T, th = (unsigned)(T >> 32);
        unsigned spins = 0;
        for (;;) {
          asm volatile("" ::: "memory");
          const u32x4 f0 = LD16(WS_FLAG + l * 32), f1 = LD16(WS_FLAG + l * 32 + 16);
          const bool ok = f0[0] == tl && f0[1] == th && f0[2] == tl && f0[3] == th && f1[0] == tl && f1[1] == th && f1[2] == tl && f1[3] == th;
          if (__all(ok) || ++spins > SPIN_CAP) break;
        }
        cfence();
      }
      lds_arrive(cnt + 7, l);
#pragma unroll
      for (int i = 0; i < 7; ++i) *(LAS u32x4*)(L + O_W2 + (8 * i + (l >> 3)) * W2_STR + (l & 7) * 16) = w2r[i];
      if (ffn) {
        const int po = WS_IN + ffn_wg(cb * 16 + (l >> 2)) * IN_STR + IN_GU + rb * 64 + (l & 3) * 16;
        u32x4 g0, g1;
        unsigned spins = 0;
        for (;;) {
          asm volatile("" ::: "memory");
          g0 = LD16(po); g1 = LD16(po + 1024);
          if (__all(ok4(g0) && ok4(g1)) || ++spins > SPIN_CAP) break;
          __builtin_amdgcn_s_sleep(8);
        }
        float hv[4];
#pragma unroll
        for (int n = 0; n < 4; ++n) {
          float tg = asf(g0[n]), tu = asf(g1[n]);
          tg += dppf<ROR4>(tg); tg += dppf<ROR8>(tg); tg = rbf(xrow_sum(tg));
          tu += dppf<ROR4>(tu); tu += dppf<ROR8>(tu); tu = rbf(xrow_sum(tu));
          hv[n] = rbf(silu(tg)) * tu;
        }
        u32x4 hp;
#pragma unroll
        for (int n = 0; n < 4; ++n) { const unsigned u = pack2(hv[n], dppf<XOR2>(hv[n])); hp[n] = u == INVAL ? 0x7FC07FC0u : u; }
        if (l < 2) ST16(hp, WS_HB + fs * 32 + l * 16);
      }
      {
        u32x4 hg;
        unsigned spins = 0;
        for (;;) {
          asm volatile("" ::: "memory");
          hg = LD16(l < 14 ? WS_HB + j * 224 + l * 16 : OOB);
          if (__all(ok4(hg)) || ++spins > SPIN_CAP) break;
          __builtin_amdgcn_s_sleep(8);
        }
        if (l < 16) {
          *(LAS u32x2*)(L + O_H + l * 8) = u32x2{__builtin_amdgcn_perm(hg[1], hg[0], 0x05040100u), __builtin_amdgcn_perm(hg[3], hg[2], 0x05040100u)};
          *(LAS u32x2*)(L + O_H + 128 + l * 8) = u32x2{__builtin_amdgcn_perm(hg[1], hg[0], 0x07060302u), __builtin_amdgcn_perm(hg[3], hg[2], 0x07060302u)};
        }
        lds_wait();
        f32x4 o0, o1;
#pragma unroll
        for (int n = 0; n < 4; ++n) {
          f32x4 d = {0, 0, 0, 0};
#pragma unroll
          for (int ks = 0; ks < 2; ++ks) {
            const u32x4 af = *(LAS const u32x4*)(L + O_H + (l & 1) * 128 + (32 * ks + 8 * g) * 2);
            const int r0 = min(32 * ks + 8 * g + qq, 51);
            const int ro = r0 * W2_STR + (16 * n + 4 * p4) * 2;
            const u32x2 b0 = tr_read(L + O_W2 + ro), b1 = tr_read(L + O_W2 + ro + 4 * W2_STR);
            d = mfma16(af, u32x4{b0[0], b0[1], b1[0], b1[1]}, d);
          }
          o0[n] = d[0]; o1[n] = d[1];
        }
        if (l < 16) { *(LAS f32x4*)(L + O_F + l * 16) = o0; *(LAS f32x4*)(L + O_F + 256 + l * 16) = o1; }
      }
      while (__builtin_amdgcn_readfirstlane(__hip_atomic_load(cnt + 6, __ATOMIC_RELAXED, __HIP_MEMORY_SCOPE_WORKGROUP)) < 3) __builtin_amdgcn_s_sleep(8);
      if (l < 32) ST16(san4(*(LAS f32x4*)(L + O_F + l * 16)), myin + IN_F2 + l * 16);
      if (bid == 0 && l == 0) st_sc1((GAS u64*)(ws + WS_EPOCH), T);
    } else if (hw == 1) {
      const GAS unsigned char* wb = (const GAS unsigned char*)wo_ + ((size_t)(akvh * 512 + 32 * asp) * 512 + l * 8) * 2;
#pragma unroll 1
      for (int h = 0; h < 2; ++h) {
        u32x4 wr[16];
#pragma unroll
        for (int i = 0; i < 16; ++i) wr[i] = ldnt(wb + (size_t)(h * 16 + i) * 1024);
        cfence();
#pragma unroll
        for (int i = 0; i < 16; ++i) *(LAS u32x4*)(L + O_WO + (h * 16 + i) * WO_STR + l * 16) = wr[i];
      }
      lds_arrive(cnt + 8, l);
    } else if (hw == 2) {
      xv = (float)((const GAS __bf16*)x_)[b * 512 + 64 * (cw >> 4) + 16 * (l & 3) + (cw & 15)];
      asm volatile("" : "+v"(xv));
    }
    __builtin_amdgcn_s_setprio(0);
  }
  lds_wait_cnt(cnt + 8, 1);
  u32x4 wf[4];
#pragma unroll
  for (int n = 0; n < 4; ++n) {
    const int ro = (8 * g + qq) * WO_STR + (64 * w + 16 * n + 4 * p4) * 2;
    const u32x2 b0 = tr_read(L + O_WO + ro), b1 = tr_read(L + O_WO + ro + 4 * WO_STR);
    wf[n] = u32x4{b0[0], b0[1], b1[0], b1[1]};
  }
  wg_barrier();
  {
    const u32x4 af = *(LAS const u32x4*)(L + O_O + g * 16);
    f32x4 so;
#pragma unroll
    for (int n = 0; n < 4; ++n) so[n] = mfma16(af, wf[n], f32x4{0, 0, 0, 0})[0];
    if (l < 16) ST16(san4(so), myin + IN_H2 + (16 * w + l) * 16);
  }
  if (w == 7) {
    const int fo = l < 32 ? WS_IN + ((l << 3) | kvh) * IN_STR + IN_F2 + (b * 16 + sp) * 16 : OOB;
    u32x4 fp = LD16(fo);
    unsigned spins = 0;
    while (!__all(ok4(fp)) && ++spins <= SPIN_CAP) {
      __builtin_amdgcn_s_sleep(4);
      asm volatile("" ::: "memory");
      const bool k0 = ok4(fp);
      const u32x4 n0 = LD16(k0 ? OOB : fo);
      fp = k0 ? fp : n0;
    }
    float f[4];
#pragma unroll
    for (int n = 0; n < 4; ++n) f[n] = wave_sum(asf(fp[n]));
    const float fv = l == 0 ? f[0] : l == 1 ? f[1] : l == 2 ? f[2] : f[3];
    if (l < 4) ((LAS float*)(L + O_FG))[l] = rbf(silu(rbf(fv)));
    lds_arrive(cnt + 5, l);
  }
  if (w == 6) {
    const int a0 = WS_IN + (b * 128 + l) * IN_STR + IN_H2 + cw * 16, a1 = a0 + 64 * IN_STR;
    u32x4 s0 = {INVAL, INVAL, INVAL, INVAL}, s1 = s0;
    u32x4 pa0 = LD16(a0), pa1 = LD16(a1);
    __builtin_amdgcn_s_sleep(PSTAG);
    for (unsigned spins = 0; spins <= SPIN_CAP; ++spins) {
      asm volatile("" ::: "memory");
      const bool k0 = ok4(s0), k1 = ok4(s1);
      const u32x4 pb0 = LD16(k0 ? OOB : a0), pb1 = LD16(k1 ? OOB : a1);
      cfence();
      s0 = k0 ? s0 : pa0; s1 = k1 ? s1 : pa1;
      const bool j0 = ok4(s0), j1 = ok4(s1);
      if (__all(j0 && j1)) break;
      asm volatile("" ::: "memory");
      pa0 = LD16(j0 ? OOB : a0); pa1 = LD16(j1 ? OOB : a1);
      cfence();
      s0 = j0 ? s0 : pb0; s1 = j1 ? s1 : pb1;
      if (__all(ok4(s0) && ok4(s1))) break;
    }
    float f[4];
#pragma unroll
    for (int n = 0; n < 4; ++n) f[n] = wave_sum(asf(s0[n]) + asf(s1[n]));
    const float s = l == 0 ? f[0] : l == 1 ? f[1] : l == 2 ? f[2] : f[3];
    lds_wait_cnt(cnt + 5, 1);
    if (l < 4) ((GAS __bf16*)out_)[b * 512 + 64 * (cw >> 4) + 16 * l + (cw & 15)] = (__bf16)(xv + rbf(rbf(s) * ((LAS float*)(L + O_FG))[l]));
  }
}

extern "C" void kernel_launch(void* const* d_in, const int* in_sizes, int n_in, void* d_out, int out_size, void* d_ws, size_t ws_size,
                              hipStream_t stream) {
  k_fused<<<NWG, NTHR, 0, stream>>>((const __hip_bfloat16*)d_in[0], (const __hip_bfloat16*)d_in[1], (const __hip_bfloat16*)d_in[2],
                                    (const __hip_bfloat16*)d_in[3], (const __hip_bfloat16*)d_in[4], (const __hip_bfloat16*)d_in[6],
                                    (const __hip_bfloat16*)d_in[7], (const __hip_bfloat16*)d_in[8], (unsigned char*)d_ws,
                                    (__hip_bfloat16*)d_out);
}
```
